```python
import torch

def _softmax(x, axis=-1):
    return torch.softmax(x, dim=axis)
def _gelu(x):
    return torch.nn.functional.gelu(x, approximate="tanh")
def _silu(x):
    return torch.nn.functional.silu(x)
def _sigmoid(x):
    return torch.sigmoid(x)
def _layernorm(x, axis=-1, eps=1e-5):
    m = x.mean(dim=axis, keepdim=True)
    v = x.var(dim=axis, keepdim=True, unbiased=False)
    return (x - m) / torch.sqrt(v + eps)
def _scatter_add(tgt, idx, src, axis=0):
    return tgt.clone().index_add_(axis, idx, src)
def _segment_sum(src, seg, n):
    out = torch.zeros((n,) + src.shape[1:], dtype=src.dtype)
    return out.index_add_(0, seg, src)
def _pad(x, pw):
    flat = tuple(v for pair in reversed(pw) for v in pair)
    return torch.nn.functional.pad(x, flat)

kernel_name = 'parallel_sum__gqa_block_cached__glu_gated_swiglu__decode'

def setup_inputs():

    _w = (lambda a: (lambda t: t.to(torch.bfloat16) if t.is_floating_point() else t)(torch.as_tensor(a)))
    def _A_setup():
        B, Sq, D = 1, 1, 512
        Hq, Hkv, Sk, Dh = 32, 8, 65536, 128
        assert Hq % Hkv == 0, (Hq, Hkv)
        return {
            "x": _w((lambda *s: torch.randn(*s, dtype=torch.bfloat16) * 0.05)(B, Sq, D)),
            "k_cache": _w((lambda *s: torch.randn(*s, dtype=torch.bfloat16) * 3.2)(B, Hkv, Sk, Dh)),
            "v_cache": _w((lambda *s: torch.randn(*s, dtype=torch.bfloat16) * 1.6)(B, Hkv, Sk, Dh)),
            "wq": _w((lambda *s: torch.randn(*s, dtype=torch.bfloat16) * 3.2)(D, Hq * Dh) * (D ** -0.5)),
            "wo": _w((lambda *s: torch.randn(*s, dtype=torch.bfloat16) * 1.6)(Hq * Dh, D) * ((Hq * Dh) ** -0.5)),
        }
    _A_in = _A_setup()

    def _B_setup():
        B, S, D, F = 1, 1, 512, 1792
        return {
            "x": _w((lambda *s: torch.randn(*s, dtype=torch.bfloat16) * 0.05)(B, S, D)),
            "w1": _w((lambda *s: torch.randn(*s, dtype=torch.bfloat16) * 1.6)(D, F) * (2.0 / D) ** 0.5),
            "w3": _w((lambda *s: torch.randn(*s, dtype=torch.bfloat16) * 1.6)(D, F) * (2.0 / D) ** 0.5),
            "w2": _w((lambda *s: torch.randn(*s, dtype=torch.bfloat16) * 1.6)(F, D) * (2.0 / F) ** 0.5),
        }
    _B_in = _B_setup()

    return {
        "A_x": _A_in["x"],
        "A_k_cache": _A_in["k_cache"],
        "A_v_cache": _A_in["v_cache"],
        "A_wq": _A_in["wq"],
        "A_wo": _A_in["wo"],
        "B_x": _B_in["x"],
        "B_w1": _B_in["w1"],
        "B_w3": _B_in["w3"],
        "B_w2": _B_in["w2"],
    }

def reference(A_x, A_k_cache, A_v_cache, A_wq, A_wo, B_x, B_w1, B_w3, B_w2):
    def _A_ref(x, k_cache, v_cache, wq, wo):
        B, Sq, D = x.shape
        Hq, Dh = 32, 128
        Hkv = k_cache.shape[1]
        rep = Hq // Hkv
        q = (lambda x, *d: x.permute(*d))((x @ wq).reshape(B, Sq, Hq, Dh), 0, 2, 1, 3)
        k = (lambda x, n, axis: torch.repeat_interleave(x, n, dim=axis))(k_cache, rep, axis=1)
        v = (lambda x, n, axis: torch.repeat_interleave(x, n, dim=axis))(v_cache, rep, axis=1)
        scores = torch.einsum("bhqd,bhkd->bhqk", q, k) * (Dh ** -0.5)
        p = _softmax(scores, axis=-1)
        o = torch.einsum("bhqk,bhkd->bhqd", p, v)
        return (lambda x, *d: x.permute(*d))(o, 0, 2, 1, 3).reshape(B, Sq, Hq * Dh) @ wo

    def _B_ref(x, w1, w3, w2):
        gate = torch.einsum("bsd,df->bsf", x, w1)
        up = torch.einsum("bsd,df->bsf", x, w3)
        h = _silu(gate) * up
        out = torch.einsum("bsf,fd->bsd", h, w2)
        return out

    x = A_x
    xn = _layernorm(x, axis=-1)
    return x + _A_ref(xn, A_k_cache, A_v_cache, A_wq, A_wo) + _B_ref(xn, B_w1, B_w3, B_w2)
```

```cpp
#include <hip/hip_runtime.h>
#include <hip/hip_bf16.h>
#include <cstdint>

#define MIMIC 1
typedef unsigned short u16;
typedef unsigned v4u __attribute__((ext_vector_type(4)));
typedef float f32x4 __attribute__((ext_vector_type(4)));
typedef __bf16 bf16x2 __attribute__((ext_vector_type(2)));

constexpr int D = 512, HQ = 32, HKV = 8, SK = 65536, DH = 128, F = 1792;
constexpr int NSPLIT = 64;
constexpr int TOK_PER_SPLIT = SK / NSPLIT;

__device__ __forceinline__ float bf2f(u16 b) { return __builtin_bit_cast(float, (unsigned)b << 16); }
__device__ __forceinline__ float rbf(float f) { return __bfloat162float(__float2bfloat16(f)); }
__device__ __forceinline__ u16 f2bf(float f) { __hip_bfloat16 h = __float2bfloat16(f); return __builtin_bit_cast(u16, h); }
__device__ __forceinline__ float dot2(unsigned k, unsigned q, float acc) {
  return __builtin_amdgcn_fdot2_f32_bf16(__builtin_bit_cast(bf16x2, k), __builtin_bit_cast(bf16x2, q), acc, false);
}
template <int CTRL> __device__ __forceinline__ float dpp(float x) {
  return __builtin_bit_cast(float, __builtin_amdgcn_mov_dpp(__builtin_bit_cast(int, x), CTRL, 0xf, 0xf, true));
}
constexpr int XOR1 = 0xB1, XOR2 = 0x4E, XOR7 = 0x141, XOR8 = 0x128, ROR4 = 0x124;
__device__ __forceinline__ float xrow16_max(float x) {
  auto s = __builtin_amdgcn_permlane16_swap(__float_as_uint(x), __float_as_uint(x), false, false);
  x = fmaxf(__uint_as_float(s[0]), __uint_as_float(s[1]));
  auto t = __builtin_amdgcn_permlane32_swap(__float_as_uint(x), __float_as_uint(x), false, false);
  return fmaxf(__uint_as_float(t[0]), __uint_as_float(t[1]));
}
__device__ __forceinline__ float xrow16_sum(float x) {
  auto s = __builtin_amdgcn_permlane16_swap(__float_as_uint(x), __float_as_uint(x), false, false);
  x = __uint_as_float(s[0]) + __uint_as_float(s[1]);
  auto t = __builtin_amdgcn_permlane32_swap(__float_as_uint(x), __float_as_uint(x), false, false);
  return __uint_as_float(t[0]) + __uint_as_float(t[1]);
}

__device__ __forceinline__ void layernorm_to_lds(const u16* x, float* xn, float* red) {
  const int tid = threadIdx.x;
  float a = bf2f(x[tid]), b = bf2f(x[tid + 256]);
  float s = a + b;
  for (int m = 32; m >= 1; m >>= 1) s += __shfl_xor(s, m);
  if ((tid & 63) == 0) red[tid >> 6] = s;
  __syncthreads();
  float mean = (red[0] + red[1] + red[2] + red[3]) * (1.f / 512.f);
  __syncthreads();
  float da = a - mean, db = b - mean;
  float v = da * da + db * db;
  for (int m = 32; m >= 1; m >>= 1) v += __shfl_xor(v, m);
  if ((tid & 63) == 0) red[tid >> 6] = v;
  __syncthreads();
  float var = (red[0] + red[1] + red[2] + red[3]) * (1.f / 512.f);
  float mb = rbf(mean), vb = rbf(var);
  float den = rbf(sqrtf(rbf(vb + 1e-5f)));
  xn[tid] = rbf(rbf(a - mb) / den);
  xn[tid + 256] = rbf(rbf(b - mb) / den);
  __syncthreads();
}

__global__ void __launch_bounds__(256) k_proj(const u16* x, const u16* wq, const u16* w1, const u16* w3, u16* q, float* gate, float* up) {
  __shared__ float xn[512]; __shared__ float red[4]; __shared__ float part[32][65];
  layernorm_to_lds(x, xn, red);
  const int b = blockIdx.x, tid = threadIdx.x;
  const u16* W; int ld, c0, kind;
  if (b < 64) { W = wq; ld = 4096; c0 = b * 64; kind = 0; }
  else if (b < 64 + 28) { W = w1; ld = F; c0 = (b - 64) * 64; kind = 1; }
  else { W = w3; ld = F; c0 = (b - 92) * 64; kind = 2; }
  const int cl = tid & 7, rl = tid >> 3;
  float acc[8] = {0, 0, 0, 0, 0, 0, 0, 0};
#pragma unroll 4
  for (int r = rl; r < 512; r += 32) {
    v4u w = *(const v4u*)(W + (size_t)r * ld + c0 + cl * 8);
    float xv = xn[r];
#pragma unroll
    for (int k = 0; k < 4; ++k) { acc[2 * k] += xv * __builtin_bit_cast(float, w[k] << 16); acc[2 * k + 1] += xv * __builtin_bit_cast(float, w[k] & 0xffff0000u); }
  }
#pragma unroll
  for (int k = 0; k < 8; ++k) part[rl][cl * 8 + k] = acc[k];
  __syncthreads();
  if (tid < 64) {
    float s = 0.f;
    for (int r = 0; r < 32; ++r) s += part[r][tid];
    if (kind == 0) q[c0 + tid] = f2bf(s);
    else if (kind == 1) gate[c0 + tid] = rbf(s);
    else up[c0 + tid] = rbf(s);
  }
}

__global__ void __launch_bounds__(256) k_attn(const u16* q, const u16* kc, const u16* vc, float* pml, float* po) {
  __shared__ __attribute__((aligned(16))) float lds[4 * 64 + 4 * 4 * (4 + DH)];
  const int s = blockIdx.x, kvh = blockIdx.y;
  const int tid = threadIdx.x, lane = tid & 63, wave = __builtin_amdgcn_readfirstlane(tid >> 6), r = lane >> 4, c = lane & 15;
  constexpr float SCALE = 0.088388347648318440f;
  constexpr float L2E = 1.4426950408889634f;
  constexpr int NP = TOK_PER_SPLIT / 16 / 4;
  v4u qpk[4];
#pragma unroll
  for (int g = 0; g < 4; ++g) qpk[g] = *(const v4u*)(q + (kvh * 4 + g) * DH + 8 * c);
  const size_t base = ((size_t)kvh * SK + (size_t)s * TOK_PER_SPLIT) * DH + (size_t)r * DH + 8 * c;
  v4u Kn[4], Vn[4];
#define ISSUE(i_) do { const size_t o_ = base + (size_t)(wave + 4 * (i_)) * 16 * DH; \
    _Pragma("unroll") for (int t_ = 0; t_ < 4; ++t_) { Kn[t_] = __builtin_nontemporal_load((const v4u*)(kc + o_ + t_ * 4 * DH)); Vn[t_] = __builtin_nontemporal_load((const v4u*)(vc + o_ + t_ * 4 * DH)); } } while (0)
  float m = -INFINITY, l = 0.f, acc[32];
#pragma unroll
  for (int k = 0; k < 32; ++k) acc[k] = 0.f;
  ISSUE(0);
  for (int i = 0; i < NP; ++i) {
    v4u Kc[4], Vc[4];
#pragma unroll
    for (int t = 0; t < 4; ++t) { Kc[t] = Kn[t]; Vc[t] = Vn[t]; }
    if (i + 1 < NP) ISSUE(i + 1);
    float x[16];
#pragma unroll
    for (int t = 0; t < 4; ++t)
#pragma unroll
      for (int g = 0; g < 4; ++g) { float d = 0.f;
#pragma unroll
        for (int k = 0; k < 4; ++k) d = dot2(Kc[t][k], qpk[g][k], d);
        x[t * 4 + g] = d; }
#define TR_STEP(HALF, CTRL, BIT) _Pragma("unroll") for (int h = 0; h < (HALF); ++h) { \
      const float lo_ = x[h], hi_ = x[h + (HALF)]; const float t1_ = lo_ + dpp<CTRL>(lo_), t2_ = hi_ + dpp<CTRL>(hi_); \
      x[h] = (c & (BIT)) ? t2_ : t1_; }
    TR_STEP(8, XOR8, 8) TR_STEP(4, XOR7, 4) TR_STEP(2, XOR2, 2) TR_STEP(1, XOR1, 1)
#undef TR_STEP
    const float sc = rbf(rbf(x[0]) * SCALE) * L2E;
    float pm = sc; pm = fmaxf(pm, dpp<ROR4>(pm)); pm = fmaxf(pm, dpp<XOR8>(pm)); pm = xrow16_max(pm);
    const float mn = fmaxf(m, pm);
    const float alpha = __builtin_amdgcn_exp2f(m - mn), p = __builtin_amdgcn_exp2f(sc - mn);
    l = fmaf(l, alpha, p); m = mn;
#pragma unroll
    for (int q4 = 0; q4 < 4; ++q4) {
      const float al = q4 == 0 ? dpp<0x00>(alpha) : q4 == 1 ? dpp<0x55>(alpha) : q4 == 2 ? dpp<0xAA>(alpha) : dpp<0xFF>(alpha);
#pragma unroll
      for (int k = 0; k < 8; ++k) acc[q4 * 8 + k] *= al; }
    float* prow = lds + (wave * 4 + r) * 16;
    prow[c] = p;
    asm volatile("s_waitcnt lgkmcnt(0)" ::: "memory");
    f32x4 pr[4];
#pragma unroll
    for (int u = 0; u < 4; ++u) pr[u] = *reinterpret_cast<const f32x4*>(prow + 4 * u);
#pragma unroll
    for (int t = 0; t < 4; ++t) {
      float vf[8];
#pragma unroll
      for (int k = 0; k < 4; ++k) { vf[2 * k] = __builtin_bit_cast(float, Vc[t][k] << 16); vf[2 * k + 1] = __builtin_bit_cast(float, Vc[t][k] & 0xffff0000u); }
#pragma unroll
      for (int g = 0; g < 4; ++g) { const float pg_ = pr[t][g];
#pragma unroll
        for (int k = 0; k < 8; ++k) acc[g * 8 + k] = fmaf(pg_, vf[k], acc[g * 8 + k]); } }
  }
#undef ISSUE
#pragma unroll
  for (int k = 0; k < 32; ++k) acc[k] = xrow16_sum(acc[k]);
  { float t = l; t += dpp<ROR4>(t); t += dpp<XOR8>(t); l = xrow16_sum(t); }
  float* mrg = lds + 256;
  constexpr int REC = 4 + DH;
  if (r == 0) {
#pragma unroll
    for (int g = 0; g < 4; ++g) {
      float* rec = mrg + (wave * 4 + g) * REC;
#pragma unroll
      for (int k = 0; k < 8; k += 4) *reinterpret_cast<f32x4*>(rec + 4 + 8 * c + k) = f32x4{acc[g * 8 + k], acc[g * 8 + k + 1], acc[g * 8 + k + 2], acc[g * 8 + k + 3]};
      if (c == g) { rec[0] = m; rec[1] = l; } } }
  __syncthreads();
  {
    const int g = wave, d0 = 2 * lane;
    const int row = kvh * 4 + g;
    float M = -INFINITY;
#pragma unroll
    for (int w = 0; w < 4; ++w) M = fmaxf(M, mrg[(w * 4 + g) * REC]);
    float L = 0.f, o0 = 0.f, o1 = 0.f;
#pragma unroll
    for (int w = 0; w < 4; ++w) { const float* rec = mrg + (w * 4 + g) * REC; const float lw = rec[1];
      const float wt = __builtin_amdgcn_exp2f(rec[0] - M);
      L = fmaf(wt, lw, L); o0 = fmaf(wt, rec[4 + d0], o0); o1 = fmaf(wt, rec[4 + d0 + 1], o1); }
    const size_t slot = (size_t)row * NSPLIT + s;
    *reinterpret_cast<float2*>(po + slot * DH + d0) = make_float2(o0, o1);
    if (lane == 0) *reinterpret_cast<float2*>(pml + 2 * slot) = make_float2(M, L);
  }
}

__global__ void __launch_bounds__(64) k_comb(const float* pml, const float* po, const float* gate, const float* up, float* o, float* h) {
  const int b = blockIdx.x, lane = threadIdx.x;
  if (b < 32) {
    const float* ml = pml + (size_t)b * NSPLIT * 2; const float* p = po + (size_t)b * NSPLIT * DH;
    float M = -INFINITY;
    for (int s = 0; s < NSPLIT; ++s) M = fmaxf(M, ml[2 * s]);
    float L = 0.f, o0 = 0.f, o1 = 0.f;
    for (int s = 0; s < NSPLIT; ++s) { const float w = __builtin_amdgcn_exp2f(ml[2 * s] - M);
      const float2 a = *reinterpret_cast<const float2*>(p + (size_t)s * DH + 2 * lane);
      L = fmaf(w, ml[2 * s + 1], L); o0 = fmaf(w, a.x, o0); o1 = fmaf(w, a.y, o1); }
    o[b * DH + 2 * lane] = rbf(o0 / L); o[b * DH + 2 * lane + 1] = rbf(o1 / L);
  } else {
    const int i = (b - 32) * 64 + lane;
    const float g = gate[i];
    const float sg = rbf(g / (1.f + __expf(-g)));
    h[i] = rbf(sg * up[i]);
  }
}
__global__ void __launch_bounds__(256) k_out(const u16* x, const float* o, const float* h, const u16* wo, const u16* w2, u16* out) {
  __shared__ float part[32][65];
  const int b = blockIdx.x, tid = threadIdx.x, cl = tid & 7, rl = tid >> 3, c0 = b * 64;
  float acc[8] = {0, 0, 0, 0, 0, 0, 0, 0};
#pragma unroll 4
  for (int r = rl; r < 4096; r += 32) {
    v4u w = *(const v4u*)(wo + (size_t)r * D + c0 + cl * 8); float xv = o[r];
#pragma unroll
    for (int k = 0; k < 4; ++k) { acc[2 * k] += xv * __builtin_bit_cast(float, w[k] << 16); acc[2 * k + 1] += xv * __builtin_bit_cast(float, w[k] & 0xffff0000u); }
  }
#pragma unroll
  for (int k = 0; k < 8; ++k) part[rl][cl * 8 + k] = acc[k];
  __syncthreads();
  float A = 0.f;
  if (tid < 64) for (int r = 0; r < 32; ++r) A += part[r][tid];
  __syncthreads();
#pragma unroll
  for (int k = 0; k < 8; ++k) acc[k] = 0.f;
#pragma unroll 4
  for (int r = rl; r < F; r += 32) {
    v4u w = *(const v4u*)(w2 + (size_t)r * D + c0 + cl * 8); float xv = h[r];
#pragma unroll
    for (int k = 0; k < 4; ++k) { acc[2 * k] += xv * __builtin_bit_cast(float, w[k] << 16); acc[2 * k + 1] += xv * __builtin_bit_cast(float, w[k] & 0xffff0000u); }
  }
#pragma unroll
  for (int k = 0; k < 8; ++k) part[rl][cl * 8 + k] = acc[k];
  __syncthreads();
  if (tid < 64) {
    float Bv = 0.f;
    for (int r = 0; r < 32; ++r) Bv += part[r][tid];
    float xv = bf2f(x[c0 + tid]);
    out[c0 + tid] = f2bf(rbf(xv + rbf(A)) + rbf(Bv));
  }
}

extern "C" void kernel_launch(void* const* d_in, const int* in_sizes, int n_in,
                              void* d_out, int out_size, void* d_ws, size_t ws_size,
                              hipStream_t stream) {
  const u16* x = (const u16*)d_in[0]; const u16* kc = (const u16*)d_in[1]; const u16* vc = (const u16*)d_in[2];
  const u16* wq = (const u16*)d_in[3]; const u16* wo = (const u16*)d_in[4];
  const u16* w1 = (const u16*)d_in[6]; const u16* w3 = (const u16*)d_in[7]; const u16* w2 = (const u16*)d_in[8];
  char* ws = (char*)d_ws;
  u16* q = (u16*)ws;
  float* gate = (float*)(ws + 16384);
  float* up = (float*)(ws + 32768);
  float* o = (float*)(ws + 49152);
  float* h = (float*)(ws + 65536);
  float* pml = (float*)(ws + 81920);
  float* po = (float*)(ws + 131072);
  k_proj<<<64 + 56, 256, 0, stream>>>(x, wq, w1, w3, q, gate, up);
  k_attn<<<dim3(NSPLIT, HKV), 256, 0, stream>>>(q, kc, vc, pml, po);
  k_comb<<<32 + 28, 64, 0, stream>>>(pml, po, gate, up, o, h);
  k_out<<<8, 256, 0, stream>>>(x, o, h, wo, w2, (u16*)d_out);
}
```

```cpp
#include <hip/hip_runtime.h>
#include <hip/hip_bf16.h>
#include <cstdint>

#define GAS __attribute__((address_space(1)))
#define LAS __attribute__((address_space(3)))
typedef unsigned short u16;
typedef unsigned long long u64;
typedef unsigned v4u __attribute__((ext_vector_type(4)));
typedef unsigned v2u __attribute__((ext_vector_type(2)));
typedef float f32x4 __attribute__((ext_vector_type(4)));
typedef __bf16 bf16x8 __attribute__((ext_vector_type(8)));
typedef __bf16 bf16x4 __attribute__((ext_vector_type(4)));
typedef GAS unsigned gu32;
typedef GAS u64 gu64;
#define RLX_AGENT __ATOMIC_RELAXED, __HIP_MEMORY_SCOPE_AGENT

constexpr int SK = 65536, FF = 1792;
constexpr int BLK = 1056, SLOT = 16 * BLK, NSLOT = 9, NFILL = 65, WOF = 60;
constexpr int L_CTRL = NSLOT * SLOT;
constexpr int L_FULL = L_CTRL, L_FREE = L_CTRL + 64, L_MISC = L_CTRL + 128;
constexpr int L_QP = L_CTRL + 256;
constexpr int L_QL = L_CTRL + 512;
constexpr int L_MRG = L_CTRL + 2048;
constexpr int L_ML = L_MRG + 8192;
constexpr int L_TOTAL = L_ML + 128;
static_assert(L_TOTAL <= 163840, "lds");
constexpr int M_CLR = 0, M_QCNT = 1, M_QRDY = 2, M_DONE = 3, M_TAGLO = 4, M_TAGHI = 5, M_CHK = 6;
constexpr int L_RED = 2 * SLOT;
constexpr int L_RED2 = L_RED + 4096;
constexpr int L_WO = (WOF % NSLOT) * SLOT;
constexpr size_t W_E = 0;
constexpr size_t W_QG = 4096;
constexpr size_t W_HG = W_QG + 32768;
constexpr size_t W_BP = W_HG + 8192;
constexpr size_t W_AP = W_BP + 65536;
constexpr size_t W_PART = W_AP + 65536;
constexpr size_t W_END = W_PART + (size_t)32 * 32 * 528;
constexpr unsigned SENT = 0xFFFFFFFFu;
constexpr unsigned SPIN_CAP = 1u << 20;

__device__ __forceinline__ float bf2f(unsigned b) { return __builtin_bit_cast(float, b << 16); }
__device__ __forceinline__ float bfhi(unsigned w) { return __builtin_bit_cast(float, w & 0xffff0000u); }
__device__ __forceinline__ unsigned cvtpk(float lo, float hi) {
  typedef __bf16 bf16x2 __attribute__((ext_vector_type(2)));
  typedef float f32x2 __attribute__((ext_vector_type(2)));
  f32x2 v = {lo, hi};
  return __builtin_bit_cast(unsigned, __builtin_convertvector(v, bf16x2));
}
__device__ __forceinline__ float rbf(float f) { return bf2f(cvtpk(f, 0.f) & 0xffffu); }
__device__ __forceinline__ unsigned f2bf(float f) { return cvtpk(f, 0.f) & 0xffffu; }
__device__ __forceinline__ unsigned sane(float f) { const unsigned u = __builtin_bit_cast(unsigned, f); return u == SENT ? 0x7FC00000u : u; }
template <int CTRL> __device__ __forceinline__ float dpp(float x) {
  return __builtin_bit_cast(float, __builtin_amdgcn_mov_dpp(__builtin_bit_cast(int, x), CTRL, 0xf, 0xf, true));
}
constexpr int ROR1 = 0x121, ROR2 = 0x122, ROR4 = 0x124, ROR8 = 0x128;
__device__ __forceinline__ float xrow_max(float x) {
  auto s = __builtin_amdgcn_permlane16_swap(__float_as_uint(x), __float_as_uint(x), false, false);
  x = fmaxf(__uint_as_float(s[0]), __uint_as_float(s[1]));
  auto t = __builtin_amdgcn_permlane32_swap(__float_as_uint(x), __float_as_uint(x), false, false);
  return fmaxf(__uint_as_float(t[0]), __uint_as_float(t[1]));
}
__device__ __forceinline__ float xrow_sum(float x) {
  auto s = __builtin_amdgcn_permlane16_swap(__float_as_uint(x), __float_as_uint(x), false, false);
  x = __uint_as_float(s[0]) + __uint_as_float(s[1]);
  auto t = __builtin_amdgcn_permlane32_swap(__float_as_uint(x), __float_as_uint(x), false, false);
  return __uint_as_float(t[0]) + __uint_as_float(t[1]);
}
__device__ __forceinline__ float wave_sum(float x) {
  x += dpp<ROR1>(x); x += dpp<ROR2>(x); x += dpp<ROR4>(x); x += dpp<ROR8>(x);
  return xrow_sum(x);
}
__device__ __forceinline__ v4u ldnt(const void* p) { return __builtin_nontemporal_load((const GAS v4u*)p); }
#define LDSW() asm volatile("s_waitcnt lgkmcnt(0)" ::: "memory")
#define VMW0() asm volatile("s_waitcnt vmcnt(0)" ::: "memory")
#define GLDS(src, dst) __builtin_amdgcn_global_load_lds((const unsigned*)(src), (unsigned*)(dst), 16, 0, 2)

__device__ __forceinline__ void ln_stats(const float (&xv)[8], float& mb, float& den) {
  float s = 0.f;
#pragma unroll
  for (int i = 0; i < 8; ++i) s += xv[i];
  const float mean = wave_sum(s) * (1.f / 512.f);
  float v = 0.f;
#pragma unroll
  for (int i = 0; i < 8; ++i) { const float d = xv[i] - mean; v = fmaf(d, d, v); }
  const float var = wave_sum(v) * (1.f / 512.f);
  mb = rbf(mean);
  den = rbf(__builtin_sqrtf(rbf(rbf(var) + 1e-5f)));
}
__device__ __forceinline__ float ln_apply(float x, float mb, float den) { return rbf(rbf(x - mb) / den); }

__global__ void __launch_bounds__(512, 2) fused_kernel(const u16* __restrict__ x, const u16* __restrict__ kc, const u16* __restrict__ vc,
                                                       const u16* __restrict__ wq, const u16* __restrict__ wo, const u16* __restrict__ w1,
                                                       const u16* __restrict__ w3, const u16* __restrict__ w2, u16* __restrict__ out,
                                                       unsigned char* ws) {
  __shared__ __attribute__((aligned(16))) unsigned char lds[L_TOTAL];
  LAS unsigned char* L8 = (LAS unsigned char*)lds;
  volatile LAS unsigned* FULL = (volatile LAS unsigned*)(L8 + L_FULL);
  volatile LAS unsigned* FREE = (volatile LAS unsigned*)(L8 + L_FREE);
  volatile LAS unsigned* MISC = (volatile LAS unsigned*)(L8 + L_MISC);
  const int tid = threadIdx.x, lane = tid & 63, wave = __builtin_amdgcn_readfirstlane(tid >> 6);
  const int b = blockIdx.x, g = b & 7, s = b >> 3;
  const int hh = 4 * g + (s >> 3), cb = s & 7;
  gu64* QG = (gu64*)(ws + W_QG);
  gu32* HG = (gu32*)(ws + W_HG);
  const __amdgpu_buffer_rsrc_t rs = __builtin_amdgcn_make_buffer_rsrc(ws, 0, (int)W_END, 0x00020000);
  constexpr float SCALE = 0.08838834764831845f, L2E = 1.4426950408889634f;

  if (wave == 1 && lane < 48) ((LAS unsigned*)(L8 + L_CTRL))[lane] = 0u;

  if (wave == 0) {
    const size_t kvoff = ((size_t)g * SK + (size_t)s * 2048) * 256;
    const unsigned char* kp = (const unsigned char*)kc + kvoff + lane * 16;
    const unsigned char* vp = (const unsigned char*)vc + kvoff + lane * 16;
    const unsigned char* wop = (const unsigned char*)wo + ((size_t)(128 * hh + (lane >> 3)) * 512 + cb * 64) * 2 + (lane & 7) * 16;
    int pub = 0, slot = 0, pslot = 0;
    for (int f = 0; f < NFILL; ++f) {
      if (f == 1) { LDSW(); __builtin_amdgcn_s_barrier(); }
      if (f >= NSLOT) {
        const unsigned need = (unsigned)(f - NSLOT + 1);
        if (FREE[slot] < need) {
          VMW0();
          for (; pub < f; ++pub) { FULL[pslot] = (unsigned)(pub + 1); pslot = pslot == NSLOT - 1 ? 0 : pslot + 1; }
          while (FREE[slot] < need) __builtin_amdgcn_s_sleep(1);
        }
        asm volatile("" ::: "memory");
      }
      const int i = f - (f > WOF ? 1 : 0);
      const unsigned char* src = (f == WOF) ? wop : (((i & 1) ? vp : kp) + (size_t)(i >> 1) * 16384);
      const int sstr = (f == WOF) ? 8192 : 1024;
      unsigned char* dst = lds + slot * SLOT;
#pragma unroll
      for (int j = 0; j < 16; ++j) GLDS(src + (size_t)j * sstr, dst + j * BLK);
      slot = slot == NSLOT - 1 ? 0 : slot + 1;
      if (f + 1 - pub > 2) {
        asm volatile("s_waitcnt vmcnt(32)" ::: "memory");
        FULL[pslot] = (unsigned)(pub + 1); ++pub; pslot = pslot == NSLOT - 1 ? 0 : pslot + 1;
      }
    }
    if (pub + 2 == NFILL) {
      asm volatile("s_waitcnt vmcnt(16)" ::: "memory");
      FULL[pslot] = (unsigned)(pub + 1); ++pub; pslot = pslot == NSLOT - 1 ? 0 : pslot + 1;
    }
    VMW0();
    for (; pub < NFILL; ++pub) { FULL[pslot] = (unsigned)(pub + 1); pslot = pslot == NSLOT - 1 ? 0 : pslot + 1; }
  } else if (wave <= 4) {
    const int c = wave - 1;
    {
      const v4u xs = *(const GAS v4u*)(x + 8 * lane);
      v4u wv[4]; unsigned xr[4];
#pragma unroll
      for (int i = 0; i < 4; ++i) {
        const int r = 128 * c + 32 * i + (lane >> 1);
        wv[i] = ldnt(wq + (size_t)r * 4096 + 512 * g + 16 * s + 8 * (lane & 1));
        xr[i] = ((const GAS u16*)x)[r];
      }
      LDSW(); __builtin_amdgcn_s_barrier();
      float xv[8];
#pragma unroll
      for (int k = 0; k < 4; ++k) { xv[2 * k] = bf2f(xs[k] & 0xffffu); xv[2 * k + 1] = bfhi(xs[k]); }
      float mb, den; ln_stats(xv, mb, den);
      float acc[8] = {0.f, 0.f, 0.f, 0.f, 0.f, 0.f, 0.f, 0.f};
#pragma unroll
      for (int i = 0; i < 4; ++i) {
        const float xn = ln_apply(bf2f(xr[i]), mb, den);
#pragma unroll
        for (int k = 0; k < 4; ++k) { acc[2 * k] = fmaf(xn, bf2f(wv[i][k] & 0xffffu), acc[2 * k]); acc[2 * k + 1] = fmaf(xn, bfhi(wv[i][k]), acc[2 * k + 1]); }
      }
#pragma unroll
      for (int k = 0; k < 8; ++k) { float t = acc[k]; t += dpp<ROR2>(t); t += dpp<ROR4>(t); t += dpp<ROR8>(t); acc[k] = xrow_sum(t); }
      if (lane < 2) {
        LAS f32x4* qp = (LAS f32x4*)(L8 + L_QP + c * 64 + lane * 32);
        qp[0] = f32x4{acc[0], acc[1], acc[2], acc[3]}; qp[1] = f32x4{acc[4], acc[5], acc[6], acc[7]};
      }
      LDSW();
      unsigned old = 0;
      if (lane == 0) old = __hip_atomic_fetch_add((LAS unsigned*)(L8 + L_MISC + 4 * M_QCNT), 1u, __ATOMIC_RELAXED, __HIP_MEMORY_SCOPE_WORKGROUP);
      old = __builtin_amdgcn_readfirstlane(old);
      asm volatile("" ::: "memory");
      if (old == 3u) {
        while (MISC[M_CLR] == 0u) __builtin_amdgcn_s_sleep(1);
        asm volatile("" ::: "memory");
        const u64 tag = ((u64)MISC[M_TAGHI] << 32) | (u64)MISC[M_TAGLO];
        if (lane < 16) {
          const volatile LAS float* qp = (const volatile LAS float*)(L8 + L_QP);
          const float qv = (qp[lane] + qp[16 + lane]) + (qp[32 + lane] + qp[48 + lane]);
          __hip_atomic_store(QG + 512 * g + 16 * s + lane, (tag << 16) | (u64)f2bf(qv), RLX_AGENT);
        }
      }
    }
    const int n = lane & 15, gq = lane >> 4, la = n >> 2;
    const int kbase = n * BLK + gq * 16;
    const int vbase = (lane >> 2) * BLK + (lane & 3) * 8;
    bf16x8 qf[4];
    f32x4 O[8];
#pragma unroll
    for (int d = 0; d < 8; ++d) O[d] = f32x4{0.f, 0.f, 0.f, 0.f};
    float m = -INFINITY, l = 0.f;
#pragma unroll 1
    for (int k = 0; k < 8; ++k) {
      const int t = c + 4 * k;
      const int fK = 2 * t + (2 * t >= WOF ? 1 : 0), fV = 2 * t + 1 + (2 * t + 1 >= WOF ? 1 : 0);
      const int sK = fK % NSLOT, sV = fV % NSLOT;
      while (FULL[sK] < (unsigned)(fK + 1)) __builtin_amdgcn_s_sleep(1);
      asm volatile("" ::: "memory");
      bf16x8 kf[4][4];
#pragma unroll
      for (int a = 0; a < 4; ++a)
#pragma unroll
        for (int ks = 0; ks < 4; ++ks) kf[a][ks] = *(const LAS bf16x8*)(L8 + sK * SLOT + kbase + a * 256 + ks * 64);
      LDSW();
      FREE[sK] = (unsigned)(fK + 1);
#pragma unroll
      for (int a = 0; a < 4; ++a) asm volatile("" : "+v"(kf[a][0]), "+v"(kf[a][1]), "+v"(kf[a][2]), "+v"(kf[a][3]) :: "memory");
      while (FULL[sV] < (unsigned)(fV + 1)) __builtin_amdgcn_s_sleep(1);
      asm volatile("" ::: "memory");
      bf16x4 vf[4][8];
#pragma unroll
      for (int cc = 0; cc < 4; ++cc)
#pragma unroll
        for (int d = 0; d < 8; ++d) vf[cc][d] = __builtin_amdgcn_ds_read_tr16_b64_v4bf16((LAS bf16x4*)(L8 + sV * SLOT + vbase + cc * 256 + d * 32));
      LDSW();
      FREE[sV] = (unsigned)(fV + 1);
#pragma unroll
      for (int cc = 0; cc < 4; ++cc) asm volatile("" : "+v"(vf[cc][0]), "+v"(vf[cc][1]), "+v"(vf[cc][2]), "+v"(vf[cc][3]), "+v"(vf[cc][4]), "+v"(vf[cc][5]), "+v"(vf[cc][6]), "+v"(vf[cc][7]) :: "memory");
      if (k == 0) {
        while (MISC[M_QRDY] == 0u) __builtin_amdgcn_s_sleep(1);
        asm volatile("" ::: "memory");
#pragma unroll
        for (int ks = 0; ks < 4; ++ks) qf[ks] = *(const LAS bf16x8*)(L8 + L_QL + (n & 3) * 256 + ks * 64 + gq * 16);
      }
      f32x4 S[4];
#pragma unroll
      for (int a = 0; a < 4; ++a) {
        S[a] = f32x4{0.f, 0.f, 0.f, 0.f};
#pragma unroll
        for (int ks = 0; ks < 4; ++ks) S[a] = __builtin_amdgcn_mfma_f32_16x16x32_bf16(kf[a][ks], qf[ks], S[a], 0, 0, 0);
      }
      float sc[4];
#pragma unroll
      for (int r = 0; r < 4; ++r) {
        const float v = la == 0 ? S[0][r] : la == 1 ? S[1][r] : la == 2 ? S[2][r] : S[3][r];
        sc[r] = rbf(rbf(v) * SCALE);
      }
      float pm = fmaxf(fmaxf(sc[0], sc[1]), fmaxf(sc[2], sc[3]));
      pm = fmaxf(pm, dpp<ROR4>(pm)); pm = fmaxf(pm, dpp<ROR8>(pm)); pm = xrow_max(pm);
      const float mn = fmaxf(m, pm);
      const float alpha = __builtin_amdgcn_exp2f((m - mn) * L2E);
      m = mn;
      float p[4];
#pragma unroll
      for (int r = 0; r < 4; ++r) p[r] = __builtin_amdgcn_exp2f((sc[r] - mn) * L2E);
      l = fmaf(l, alpha, (p[0] + p[1]) + (p[2] + p[3]));
      const unsigned p01 = cvtpk(p[0], p[1]), p23 = cvtpk(p[2], p[3]);
      float al[4];
#pragma unroll
      for (int r = 0; r < 4; ++r) al[r] = __builtin_bit_cast(float, __builtin_amdgcn_readlane(__builtin_bit_cast(int, alpha), r));
#pragma unroll
      for (int d = 0; d < 8; ++d)
#pragma unroll
        for (int r = 0; r < 4; ++r) O[d][r] *= al[r];
#pragma unroll
      for (int u = 0; u < 2; ++u) {
        const v4u pa = {la == 2 * u ? p01 : 0u, la == 2 * u ? p23 : 0u, la == 2 * u + 1 ? p01 : 0u, la == 2 * u + 1 ? p23 : 0u};
        const bf16x8 pf = __builtin_bit_cast(bf16x8, pa);
#pragma unroll
        for (int d = 0; d < 8; ++d) {
          const bf16x8 vb = __builtin_shufflevector(vf[2 * u][d], vf[2 * u + 1][d], 0, 1, 2, 3, 4, 5, 6, 7);
          O[d] = __builtin_amdgcn_mfma_f32_16x16x32_bf16(pf, vb, O[d], 0, 0, 0);
        }
      }
    }
    l += dpp<ROR4>(l); l += dpp<ROR8>(l); l = xrow_sum(l);
    LAS float* mrg = (LAS float*)(L8 + L_MRG + c * 2048);
#pragma unroll
    for (int d = 0; d < 8; ++d) {
      f32x4 o;
#pragma unroll
      for (int r = 0; r < 4; ++r) o[r] = xrow_sum(O[d][r]);
      const float ov = gq == 0 ? o[0] : gq == 1 ? o[1] : gq == 2 ? o[2] : o[3];
      mrg[gq * 128 + d * 16 + n] = ov;
    }
    if (lane < 4) { LAS float* ml = (LAS float*)(L8 + L_ML + c * 32 + lane * 8); ml[0] = m; ml[1] = l; }
  } else if (wave == 5) {
    const u64 E = __hip_atomic_load((gu64*)(ws + W_E), RLX_AGENT);
    const v4u sv = {SENT, SENT, SENT, SENT};
#pragma unroll
    for (int h = 0; h < 4; ++h)
      if (lane < 33) __builtin_amdgcn_raw_buffer_store_b128(sv, rs, (int)(W_PART + ((size_t)(4 * g + h) * 32 + s) * 528) + lane * 16, 0, 16);
    if (lane < 16) __builtin_amdgcn_raw_buffer_store_b128(sv, rs, (int)(W_AP + ((size_t)hh * 512 + cb * 64) * 4) + lane * 16, 0, 16);
    else if (lane < 32) __builtin_amdgcn_raw_buffer_store_b128(sv, rs, (int)(W_BP + ((size_t)hh * 512 + cb * 64) * 4) + (lane - 16) * 16, 0, 16);
    else if (lane < 34 && s < 28) __builtin_amdgcn_raw_buffer_store_b128(sv, rs, (int)(W_HG + (size_t)(g * 28 + s) * 32) + (lane - 32) * 16, 0, 16);
    LDSW(); __builtin_amdgcn_s_barrier();
    VMW0();
    const u64 tag = (E + 1) & 0xFFFFFFFFFFFFull;
    if (lane == 0) { MISC[M_TAGLO] = (unsigned)tag; MISC[M_TAGHI] = (unsigned)(tag >> 32); }
    LDSW();
    if (lane == 0) MISC[M_CLR] = 1u;
    u64 v[8];
    for (unsigned spins = 0; spins < SPIN_CAP; ++spins) {
      bool ok = true;
#pragma unroll
      for (int i = 0; i < 8; ++i) { v[i] = __hip_atomic_load(QG + 512 * g + 64 * i + lane, RLX_AGENT); ok &= (v[i] >> 16) == tag; }
      if (__all(ok)) break;
      __builtin_amdgcn_s_sleep(1);
    }
#pragma unroll
    for (int i = 0; i < 8; ++i) ((LAS u16*)(L8 + L_QL))[64 * i + lane] = (u16)v[i];
    LDSW();
    if (lane == 0) MISC[M_QRDY] = 1u;
  } else if (wave == 6) {
    unsigned xr[8];
#pragma unroll
    for (int i = 0; i < 8; ++i) xr[i] = ((const GAS u16*)x)[lane + 64 * i];
    LDSW(); __builtin_amdgcn_s_barrier();
    if (s < 28) {
      const int t = g * 28 + s;
      float xv[8];
#pragma unroll
      for (int i = 0; i < 8; ++i) xv[i] = bf2f(xr[i]);
      float mb, den; ln_stats(xv, mb, den);
      while (MISC[M_QRDY] == 0u) __builtin_amdgcn_s_sleep(4);
      asm volatile("" ::: "memory");
      v4u a1[8], a3[8];
#pragma unroll
      for (int i = 0; i < 8; ++i) { a1[i] = ldnt(w1 + (size_t)(lane + 64 * i) * FF + 8 * t); a3[i] = ldnt(w3 + (size_t)(lane + 64 * i) * FF + 8 * t); }
      float ga[8] = {0.f, 0.f, 0.f, 0.f, 0.f, 0.f, 0.f, 0.f}, ua[8] = {0.f, 0.f, 0.f, 0.f, 0.f, 0.f, 0.f, 0.f};
#pragma unroll
      for (int i = 0; i < 8; ++i) {
        const float xn = ln_apply(xv[i], mb, den);
#pragma unroll
        for (int k = 0; k < 4; ++k) {
          ga[2 * k] = fmaf(xn, bf2f(a1[i][k] & 0xffffu), ga[2 * k]); ga[2 * k + 1] = fmaf(xn, bfhi(a1[i][k]), ga[2 * k + 1]);
          ua[2 * k] = fmaf(xn, bf2f(a3[i][k] & 0xffffu), ua[2 * k]); ua[2 * k + 1] = fmaf(xn, bfhi(a3[i][k]), ua[2 * k + 1]);
        }
      }
      float gs = 0.f, us = 0.f;
#pragma unroll
      for (int k = 0; k < 8; ++k) { const float a = wave_sum(ga[k]), bsum = wave_sum(ua[k]); if (lane == k) { gs = a; us = bsum; } }
      const float gb = rbf(gs), ub = rbf(us);
      const float sg = rbf(gb / (1.f + __expf(-gb)));
      const float hv = rbf(sg * ub);
      if (lane < 8) __hip_atomic_store(HG + 8 * t + lane, sane(hv), RLX_AGENT);
    }
  } else {
    LDSW(); __builtin_amdgcn_s_barrier();
    while (MISC[M_QRDY] == 0u) __builtin_amdgcn_s_sleep(4);
    asm volatile("" ::: "memory");
    const u64 tag = ((u64)MISC[M_TAGHI] << 32) | (u64)MISC[M_TAGLO];
    v4u wv[7];
#pragma unroll
    for (int i = 0; i < 7; ++i) wv[i] = ldnt(w2 + (size_t)(56 * hh + 8 * i + (lane >> 3)) * 512 + 64 * cb + 8 * (lane & 7));
    int pg, ps;
    if (lane < 32) { const int t = 7 * hh + (lane < 7 ? lane : 0); pg = t / 28; ps = t % 28; }
    else { const int h2 = lane - 32; pg = h2 >> 2; ps = 8 * (h2 & 3) + cb; }
    for (unsigned spins = 0; spins < SPIN_CAP; ++spins) {
      const u64 v = __hip_atomic_load(QG + 512 * pg + 16 * ps, RLX_AGENT);
      if (__all((v >> 16) == tag)) break;
      __builtin_amdgcn_s_sleep(4);
    }
    asm volatile("" ::: "memory");
    if (lane == 0) MISC[M_CHK] = 1u;
    unsigned hv = 0;
    for (unsigned spins = 0; spins < SPIN_CAP; ++spins) {
      hv = __hip_atomic_load(HG + 56 * hh + (lane < 56 ? lane : 0), RLX_AGENT);
      if (__all(hv != SENT)) break;
      __builtin_amdgcn_s_sleep(4);
    }
    float acc[8] = {0.f, 0.f, 0.f, 0.f, 0.f, 0.f, 0.f, 0.f};
#pragma unroll
    for (int i = 0; i < 7; ++i) {
      const float h = __builtin_bit_cast(float, __shfl((int)hv, 8 * i + (lane >> 3)));
#pragma unroll
      for (int k = 0; k < 4; ++k) { acc[2 * k] = fmaf(h, bf2f(wv[i][k] & 0xffffu), acc[2 * k]); acc[2 * k + 1] = fmaf(h, bfhi(wv[i][k]), acc[2 * k + 1]); }
    }
#pragma unroll
    for (int k = 0; k < 8; ++k) { float t = acc[k]; t += dpp<ROR8>(t); acc[k] = xrow_sum(t); }
    if (lane < 8) {
      const int off = (int)(W_BP + ((size_t)hh * 512 + cb * 64 + lane * 8) * 4);
      __builtin_amdgcn_raw_buffer_store_b128(v4u{sane(acc[0]), sane(acc[1]), sane(acc[2]), sane(acc[3])}, rs, off, 0, 16);
      __builtin_amdgcn_raw_buffer_store_b128(v4u{sane(acc[4]), sane(acc[5]), sane(acc[6]), sane(acc[7])}, rs, off + 16, 0, 16);
    }
  }
  __syncthreads();
  if (tid < 128) {
    const int h = tid >> 5, d4 = tid & 31;
    const LAS float* ml = (const LAS float*)(L8 + L_ML);
    float mw[4], M = -INFINITY;
#pragma unroll
    for (int w = 0; w < 4; ++w) { mw[w] = ml[w * 8 + h * 2]; M = fmaxf(M, mw[w]); }
    f32x4 o = {0.f, 0.f, 0.f, 0.f}; float L = 0.f;
#pragma unroll
    for (int w = 0; w < 4; ++w) {
      const float wt = __builtin_amdgcn_exp2f((mw[w] - M) * L2E);
      const f32x4 ov = *(const LAS f32x4*)(L8 + L_MRG + w * 2048 + h * 512 + d4 * 16);
      o += wt * ov; L = fmaf(wt, ml[w * 8 + h * 2 + 1], L);
    }
    const int off = (int)(W_PART + ((size_t)(4 * g + h) * 32 + s) * 528);
    __builtin_amdgcn_raw_buffer_store_b128(v4u{sane(o[0]), sane(o[1]), sane(o[2]), sane(o[3])}, rs, off + d4 * 16, 0, 16);
    if (d4 == 0) __builtin_amdgcn_raw_buffer_store_b128(v4u{sane(M), sane(L), 0u, 0u}, rs, off + 512, 0, 16);
  }
  {
    const int c4 = tid & 31, sg = tid >> 5;
    const int pbase = (int)(W_PART + (size_t)hh * 32 * 528);
    v4u o0, o1; v2u mlv;
    for (unsigned spins = 0; spins < SPIN_CAP; ++spins) {
      asm volatile("" ::: "memory");
      o0 = __builtin_amdgcn_raw_buffer_load_b128(rs, pbase + sg * 528 + c4 * 16, 0, 16);
      o1 = __builtin_amdgcn_raw_buffer_load_b128(rs, pbase + (sg + 16) * 528 + c4 * 16, 0, 16);
      mlv = __builtin_amdgcn_raw_buffer_load_b64(rs, pbase + (lane & 31) * 528 + 512, 0, 16);
      const bool ok = o0[0] != SENT && o0[1] != SENT && o0[2] != SENT && o0[3] != SENT && o1[0] != SENT && o1[1] != SENT && o1[2] != SENT && o1[3] != SENT && mlv[0] != SENT && mlv[1] != SENT;
      if (__all(ok)) break;
      __builtin_amdgcn_s_sleep(1);
    }
    const float ms = __uint_as_float(mlv[0]), ls = __uint_as_float(mlv[1]);
    float M = ms; M = fmaxf(M, dpp<ROR1>(M)); M = fmaxf(M, dpp<ROR2>(M)); M = fmaxf(M, dpp<ROR4>(M)); M = fmaxf(M, dpp<ROR8>(M)); M = xrow_max(M);
    const float wt = __builtin_amdgcn_exp2f((ms - M) * L2E);
    const float L = wave_sum(wt * ls) * 0.5f;
    const float w0 = __shfl(wt, sg), w1v = __shfl(wt, sg + 16);
    f32x4 o;
#pragma unroll
    for (int k = 0; k < 4; ++k) o[k] = fmaf(w0, __uint_as_float(o0[k]), w1v * __uint_as_float(o1[k]));
#pragma unroll
    for (int k = 0; k < 4; ++k) o[k] += __shfl_xor(o[k], 32);
    if (lane < 32) *(LAS f32x4*)(L8 + L_RED + wave * 512 + c4 * 16) = o;
    __syncthreads();
    const int rg = tid >> 5;
    f32x4 oa = {0.f, 0.f, 0.f, 0.f}, ob = {0.f, 0.f, 0.f, 0.f};
#pragma unroll
    for (int w = 0; w < 8; ++w) { oa += *(const LAS f32x4*)(L8 + L_RED + w * 512 + rg * 32); ob += *(const LAS f32x4*)(L8 + L_RED + w * 512 + rg * 32 + 16); }
    const float inv = 1.f / L;
    float a0 = 0.f, a1 = 0.f;
#pragma unroll
    for (int r = 0; r < 8; ++r) {
      const float ov = rbf((r < 4 ? oa[r & 3] : ob[r & 3]) * inv);
      const unsigned w = *(const LAS unsigned*)(L8 + L_WO + rg * BLK + r * 128 + c4 * 4);
      a0 = fmaf(ov, bf2f(w & 0xffffu), a0); a1 = fmaf(ov, bfhi(w), a1);
    }
    a0 += __shfl_xor(a0, 32); a1 += __shfl_xor(a1, 32);
    if (lane < 32) *(LAS v2u*)(L8 + L_RED2 + wave * 256 + c4 * 8) = v2u{__builtin_bit_cast(unsigned, a0), __builtin_bit_cast(unsigned, a1)};
    __syncthreads();
  }
  if (wave == 0) {
    if (lane < 16) {
      f32x4 a = {0.f, 0.f, 0.f, 0.f};
#pragma unroll
      for (int w = 0; w < 8; ++w) a += *(const LAS f32x4*)(L8 + L_RED2 + w * 256 + lane * 16);
      __builtin_amdgcn_raw_buffer_store_b128(v4u{sane(a[0]), sane(a[1]), sane(a[2]), sane(a[3])}, rs, (int)(W_AP + ((size_t)hh * 512 + cb * 64) * 4) + lane * 16, 0, 16);
    }
    while (MISC[M_CHK] == 0u) __builtin_amdgcn_s_sleep(1);
    asm volatile("" ::: "memory");
    const int col = 64 * cb + 2 * hh;
    const unsigned xw = *(const GAS unsigned*)(x + col);
    const int off = (int)((lane < 32 ? W_AP : W_BP) + ((size_t)(lane & 31) * 512 + col) * 4);
    v2u v;
    for (unsigned spins = 0; spins < SPIN_CAP; ++spins) {
      asm volatile("" ::: "memory");
      v = __builtin_amdgcn_raw_buffer_load_b64(rs, off, 0, 16);
      if (__all(v[0] != SENT && v[1] != SENT)) break;
      __builtin_amdgcn_s_sleep(1);
    }
    float r0 = __uint_as_float(v[0]), r1 = __uint_as_float(v[1]);
    r0 += dpp<ROR1>(r0); r0 += dpp<ROR2>(r0); r0 += dpp<ROR4>(r0); r0 += dpp<ROR8>(r0); r0 += __shfl_xor(r0, 16);
    r1 += dpp<ROR1>(r1); r1 += dpp<ROR2>(r1); r1 += dpp<ROR4>(r1); r1 += dpp<ROR8>(r1); r1 += __shfl_xor(r1, 16);
    const float B0 = __shfl(r0, 32), B1 = __shfl(r1, 32);
    if (lane == 0) {
      const float y0 = rbf(bf2f(xw & 0xffffu) + rbf(r0)) + rbf(B0);
      const float y1 = rbf(bfhi(xw) + rbf(r1)) + rbf(B1);
      *(GAS unsigned*)(out + col) = cvtpk(y0, y1);
      if (b == 0) {
        const u64 tag = ((u64)MISC[M_TAGHI] << 32) | (u64)MISC[M_TAGLO];
        __hip_atomic_store((gu64*)(ws + W_E), tag, RLX_AGENT);
      }
    }
  }
}

extern "C" void kernel_launch(void* const* d_in, const int* in_sizes, int n_in,
                              void* d_out, int out_size, void* d_ws, size_t ws_size,
                              hipStream_t stream) {
  fused_kernel<<<256, 512, 0, stream>>>((const u16*)d_in[0], (const u16*)d_in[1], (const u16*)d_in[2], (const u16*)d_in[3], (const u16*)d_in[4],
                                        (const u16*)d_in[6], (const u16*)d_in[7], (const u16*)d_in[8], (u16*)d_out, (unsigned char*)d_ws);
}
```

```cpp
#include <hip/hip_runtime.h>
#include <hip/hip_bf16.h>
#include <cstdint>

#define GAS __attribute__((address_space(1)))
#define LAS __attribute__((address_space(3)))
typedef unsigned short u16;
typedef unsigned long long u64;
typedef unsigned v4u __attribute__((ext_vector_type(4)));
typedef unsigned v2u __attribute__((ext_vector_type(2)));
typedef float f32x4 __attribute__((ext_vector_type(4)));
typedef __bf16 bf16x8 __attribute__((ext_vector_type(8)));
typedef short s16x4 __attribute__((ext_vector_type(4)));
typedef GAS unsigned gu32;
typedef GAS u64 gu64;
#define RLX_AGENT __ATOMIC_RELAXED, __HIP_MEMORY_SCOPE_AGENT

constexpr int SK = 65536, FF = 1792;
constexpr int BLK = 1056, SLOT = 16 * BLK, NSLOT = 9;
#define K_NSTAT 26
#define K_DEPTH 1
constexpr int NSTAT = K_NSTAT, NDYN = (32 - NSTAT) * 32, DEPTH = K_DEPTH;
constexpr int L_CTRL = NSLOT * SLOT;
constexpr int L_FULL = L_CTRL;
constexpr int L_MISC = L_CTRL + 64;
constexpr int L_TQ = L_CTRL + 128;
constexpr int L_FREE = L_CTRL + 160;
constexpr int L_ML = L_CTRL + 320;
constexpr int L_QP = L_CTRL + 512;
constexpr int L_QL = L_QP + 1024;
constexpr int L_XN = L_QL + 1024;
constexpr int L_MRG = L_XN + 1024;
constexpr int L_TOTAL = L_MRG + 8192;
static_assert(L_TOTAL <= 163840, "lds");
constexpr int M_CLR = 0, M_QCNT = 1, M_QRDY = 2, M_NT = 3, M_TAGLO = 4, M_TAGHI = 5, M_CHK = 6, M_LDI = 7, M_TQN = 8, M_WOS = 9, M_CCNT = 10;
constexpr unsigned TQ_END = 0xFFFFu;
constexpr size_t W_E = 0;
constexpr size_t W_CTR = 1024;
constexpr size_t W_QF = 4096;
constexpr size_t W_QPG = 8192;
constexpr size_t W_GP = W_QPG + 65536;
constexpr size_t W_BP = W_GP + 65536;
constexpr size_t W_AP = W_BP + 65536;
constexpr int PROW = 640;
constexpr size_t W_PART = W_AP + 65536;
constexpr size_t W_END = W_PART + (size_t)32 * 32 * PROW;
constexpr size_t W_STAMP = 4u << 20;
#define STAMP(k) do { } while (0)
constexpr unsigned SENT = 0xFFFFFFFFu;
constexpr unsigned SPIN_CAP = 1u << 20;

__device__ __forceinline__ float bf2f(unsigned b) { return __builtin_bit_cast(float, b << 16); }
__device__ __forceinline__ float bfhi(unsigned w) { return __builtin_bit_cast(float, w & 0xffff0000u); }
__device__ __forceinline__ unsigned cvtpk(float lo, float hi) {
  typedef __bf16 bf16x2 __attribute__((ext_vector_type(2)));
  typedef float f32x2 __attribute__((ext_vector_type(2)));
  f32x2 v = {lo, hi};
  return __builtin_bit_cast(unsigned, __builtin_convertvector(v, bf16x2));
}
__device__ __forceinline__ float rbf(float f) { return bf2f(cvtpk(f, 0.f) & 0xffffu); }
__device__ __forceinline__ unsigned sane(float f) { const unsigned u = __builtin_bit_cast(unsigned, f); return u == SENT ? 0x7FC00000u : u; }
__device__ __forceinline__ v4u sane4(f32x4 a) { return v4u{sane(a[0]), sane(a[1]), sane(a[2]), sane(a[3])}; }
__device__ __forceinline__ bool valid4(v4u a) { return a[0] != SENT && a[1] != SENT && a[2] != SENT && a[3] != SENT; }
__device__ __forceinline__ bool valid2(v2u a) { return a[0] != SENT && a[1] != SENT; }
template <int CTRL> __device__ __forceinline__ float dpp(float x) {
  return __builtin_bit_cast(float, __builtin_amdgcn_mov_dpp(__builtin_bit_cast(int, x), CTRL, 0xf, 0xf, true));
}
constexpr int ROR1 = 0x121, ROR2 = 0x122, ROR4 = 0x124, ROR8 = 0x128, NEWBCAST = 0x150;
__device__ __forceinline__ float x32_sum(float x) {
  auto t = __builtin_amdgcn_permlane32_swap(__float_as_uint(x), __float_as_uint(x), false, false);
  return __uint_as_float(t[0]) + __uint_as_float(t[1]);
}
__device__ __forceinline__ float x16_sum(float x) {
  auto t = __builtin_amdgcn_permlane16_swap(__float_as_uint(x), __float_as_uint(x), false, false);
  return __uint_as_float(t[0]) + __uint_as_float(t[1]);
}
__device__ __forceinline__ float x32_max(float x) {
  auto t = __builtin_amdgcn_permlane32_swap(__float_as_uint(x), __float_as_uint(x), false, false);
  return fmaxf(__uint_as_float(t[0]), __uint_as_float(t[1]));
}
__device__ __forceinline__ float x16_max(float x) {
  auto t = __builtin_amdgcn_permlane16_swap(__float_as_uint(x), __float_as_uint(x), false, false);
  return fmaxf(__uint_as_float(t[0]), __uint_as_float(t[1]));
}
__device__ __forceinline__ float xrow_max(float x) { return x32_max(x16_max(x)); }
__device__ __forceinline__ float xrow_sum(float x) { return x32_sum(x16_sum(x)); }
__device__ __forceinline__ float wave_sum(float x) {
  x += dpp<ROR1>(x); x += dpp<ROR2>(x); x += dpp<ROR4>(x); x += dpp<ROR8>(x);
  return xrow_sum(x);
}
__device__ __forceinline__ v4u ldnt(const void* p) { return __builtin_nontemporal_load((const GAS v4u*)p); }
#define LDSW() asm volatile("s_waitcnt lgkmcnt(0)" ::: "memory")
#define VMW0() asm volatile("s_waitcnt vmcnt(0)" ::: "memory")
#define CBAR() asm volatile("" ::: "memory")
#define GLDS(src, dst) __builtin_amdgcn_global_load_lds((const unsigned*)(src), (unsigned*)(dst), 16, 0, 2)
#define ST16(v, off) __builtin_amdgcn_raw_buffer_store_b128((v), rs, (int)(off), 0, 16)
#define LD16(off) __builtin_amdgcn_raw_buffer_load_b128(rs, (int)(off), 0, 16)
#define LD8(off) __builtin_amdgcn_raw_buffer_load_b64(rs, (int)(off), 0, 16)
#define LD4(off) __builtin_amdgcn_raw_buffer_load_b32(rs, (int)(off), 0, 16)

__device__ __forceinline__ void fma8(float (&acc)[8], float xv, v4u w) {
#pragma unroll
  for (int k = 0; k < 4; ++k) { acc[2 * k] = fmaf(xv, bf2f(w[k] & 0xffffu), acc[2 * k]); acc[2 * k + 1] = fmaf(xv, bfhi(w[k]), acc[2 * k + 1]); }
}

__global__ void __launch_bounds__(512, 2) fused_kernel(const u16* __restrict__ x, const u16* __restrict__ kc, const u16* __restrict__ vc,
                                                       const u16* __restrict__ wq, const u16* __restrict__ wo, const u16* __restrict__ w1,
                                                       const u16* __restrict__ w3, const u16* __restrict__ w2, u16* __restrict__ out,
                                                       unsigned char* ws) {
  __shared__ __attribute__((aligned(16))) unsigned char lds[L_TOTAL];
  LAS unsigned char* L8 = (LAS unsigned char*)lds;
  volatile LAS unsigned* FULL = (volatile LAS unsigned*)(L8 + L_FULL);
  volatile LAS unsigned* MISC = (volatile LAS unsigned*)(L8 + L_MISC);
  const int tid = threadIdx.x, lane = tid & 63, wave = __builtin_amdgcn_readfirstlane(tid >> 6);
  const int b = blockIdx.x, g = b >> 5, s = b & 31;
  const int hh = 4 * g + (s >> 3), cb = s & 7;
  gu64* QF = (gu64*)(ws + W_QF);
  const __amdgpu_buffer_rsrc_t rs = __builtin_amdgcn_make_buffer_rsrc(ws, 0, (int)W_END, 0x00020000);
  constexpr float SCALE = 0.08838834764831845f, L2E = 1.4426950408889634f;

  if (wave == 1) { ((LAS unsigned*)(L8 + L_CTRL))[lane] = 0u; if (lane < 16) ((LAS unsigned*)(L8 + L_CTRL))[64 + lane] = 0u; }

  if (wave == 0) {
    const size_t kvoff = (size_t)g * SK * 256;
    const unsigned char* kp = (const unsigned char*)kc + kvoff + lane * 16;
    const unsigned char* vp = (const unsigned char*)vc + kvoff + lane * 16;
    const unsigned char* wop = (const unsigned char*)wo + ((size_t)(128 * hh + (lane >> 3)) * 512 + cb * 64) * 2 + (lane & 7) * 16;
    int pub = 0, slot = 0, pslot = 0;
    unsigned tile = 0;
    LDSW(); __builtin_amdgcn_s_barrier();
#define PUBLISH_ALL(upto) do { VMW0(); for (; pub < (upto); ++pub) { FULL[pslot] = (unsigned)(pub + 1); pslot = pslot == NSLOT - 1 ? 0 : pslot + 1; } } while (0)
#define FREE_MIN(sl) ({ const v4u fr_ = *(volatile LAS v4u*)(L8 + L_FREE + (sl) * 16); min(min(fr_[0], fr_[1]), min(fr_[2], fr_[3])); })
    int f = 0;
    for (;; ++f) {
      bool last = false;
      if ((f & 1) == 0) {
        const int i = f >> 1;
        if (lane == 0) MISC[M_LDI] = (unsigned)i;
        if (i < NSTAT) tile = (unsigned)(32 * s + i);
        else {
          if ((int)MISC[M_TQN] <= i - NSTAT) {
            PUBLISH_ALL(f);
            while ((int)MISC[M_TQN] <= i - NSTAT) __builtin_amdgcn_s_sleep(1);
          }
          CBAR();
          tile = __builtin_amdgcn_readfirstlane(((volatile LAS unsigned*)(L8 + L_TQ))[(i - NSTAT) & 7]);
          last = tile == TQ_END;
          if (last && lane == 0) { MISC[M_WOS] = (unsigned)slot; MISC[M_NT] = (unsigned)(i + 1); }
        }
      }
      if (f >= NSLOT) {
        const unsigned need = (unsigned)(f - NSLOT + 1);
        if (FREE_MIN(slot) < need) {
          PUBLISH_ALL(f);
          while (FREE_MIN(slot) < need) __builtin_amdgcn_s_sleep(1);
        }
        CBAR();
      }
      const unsigned char* src = last ? wop : (((f & 1) ? vp : kp) + (size_t)tile * 16384);
      const int sstr = last ? 8192 : 1024;
      unsigned char* dst = lds + slot * SLOT;
#pragma unroll
      for (int j = 0; j < 16; ++j) GLDS(src + (size_t)j * sstr, dst + j * BLK);
      slot = slot == NSLOT - 1 ? 0 : slot + 1;
      if (f + 1 - pub > 2) {
        asm volatile("s_waitcnt vmcnt(32)" ::: "memory");
        FULL[pslot] = (unsigned)(pub + 1); ++pub; pslot = pslot == NSLOT - 1 ? 0 : pslot + 1;
      }
      if (last) break;
    }
    if (pub + 1 == f) {
      asm volatile("s_waitcnt vmcnt(16)" ::: "memory");
      FULL[pslot] = (unsigned)(pub + 1); ++pub; pslot = pslot == NSLOT - 1 ? 0 : pslot + 1;
    }
    STAMP(3);
  } else if (wave <= 4) {
    const int c = wave - 1;
    {
      const v4u xs = *(const GAS v4u*)(x + 8 * lane);
      v4u wv[4];
      const int r0 = 128 * (s >> 3) + 32 * c + (lane >> 3);
#pragma unroll
      for (int i = 0; i < 4; ++i) wv[i] = ldnt(wq + (size_t)(r0 + 8 * i) * 4096 + 512 * g + 64 * cb + 8 * (lane & 7));
      LDSW(); __builtin_amdgcn_s_barrier();
      float xv[8];
#pragma unroll
      for (int k = 0; k < 4; ++k) { xv[2 * k] = bf2f(xs[k] & 0xffffu); xv[2 * k + 1] = bfhi(xs[k]); }
      float sm = 0.f;
#pragma unroll
      for (int i = 0; i < 8; ++i) sm += xv[i];
      const float mean = wave_sum(sm) * (1.f / 512.f);
      float vv = 0.f;
#pragma unroll
      for (int i = 0; i < 8; ++i) { const float d = xv[i] - mean; vv = fmaf(d, d, vv); }
      const float var = wave_sum(vv) * (1.f / 512.f);
      const float mb = rbf(mean), den = rbf(__builtin_sqrtf(rbf(rbf(var) + 1e-5f)));
      v4u xn;
#pragma unroll
      for (int k = 0; k < 4; ++k) xn[k] = cvtpk(rbf(xv[2 * k] - mb) / den, rbf(xv[2 * k + 1] - mb) / den);
      *(LAS v4u*)(L8 + L_XN + lane * 16) = xn;
      LDSW();
      float acc[8] = {0.f, 0.f, 0.f, 0.f, 0.f, 0.f, 0.f, 0.f};
#pragma unroll
      for (int i = 0; i < 4; ++i) fma8(acc, bf2f(((const LAS u16*)(L8 + L_XN))[r0 + 8 * i]), wv[i]);
#pragma unroll
      for (int k = 0; k < 8; ++k) { float t = acc[k]; t += dpp<ROR8>(t); acc[k] = xrow_sum(t); }
      if (lane < 8) {
        LAS f32x4* qp = (LAS f32x4*)(L8 + L_QP + c * 256 + lane * 32);
        qp[0] = f32x4{acc[0], acc[1], acc[2], acc[3]}; qp[1] = f32x4{acc[4], acc[5], acc[6], acc[7]};
      }
      LDSW();
      unsigned old = 0;
      if (lane == 0) old = __hip_atomic_fetch_add((LAS unsigned*)(L8 + L_MISC + 4 * M_QCNT), 1u, __ATOMIC_RELAXED, __HIP_MEMORY_SCOPE_WORKGROUP);
      old = __builtin_amdgcn_readfirstlane(old);
      CBAR();
      if (old == 3u) {
        if (lane < 16) {
          const LAS f32x4* qp = (const LAS f32x4*)(L8 + L_QP + lane * 16);
          const f32x4 qv = (qp[0] + qp[16]) + (qp[32] + qp[48]);
          ST16(__builtin_bit_cast(v4u, qv), W_QPG + ((size_t)(g * 4 + (s >> 3)) * 512 + 64 * cb + 4 * lane) * 4);
        }
        VMW0();
        while (MISC[M_CLR] == 0u) __builtin_amdgcn_s_sleep(1);
        CBAR();
        const u64 tag = ((u64)MISC[M_TAGHI] << 32) | (u64)MISC[M_TAGLO];
        if (lane == 0) __hip_atomic_store(QF + 32 * g + s, tag, RLX_AGENT);
        STAMP(1);
      }
    }
    const int n = lane & 15, gq = lane >> 4;
    const int kbase = n * BLK + c * 256 + gq * 16;
    const int vbase = (lane >> 2) * BLK + c * 256 + (lane & 3) * 8;
    volatile LAS unsigned* FREEC = (volatile LAS unsigned*)(L8 + L_FREE + c * 4);
    bf16x8 qf[4];
    f32x4 O[8];
#pragma unroll
    for (int d = 0; d < 8; ++d) O[d] = f32x4{0.f, 0.f, 0.f, 0.f};
    float m = -INFINITY, l = 0.f;
#define READ_K(kf, fK) do { const int sK_ = (fK) % NSLOT; \
      _Pragma("unroll") for (int ks = 0; ks < 4; ++ks) kf[ks] = *(const LAS bf16x8*)(L8 + sK_ * SLOT + kbase + ks * 64); \
      LDSW(); FREEC[sK_ * 4] = (unsigned)((fK) + 1); \
      asm volatile("" : "+v"(kf[0]), "+v"(kf[1]), "+v"(kf[2]), "+v"(kf[3]) :: "memory"); } while (0)
#define READ_V(vf, fV) do { const int sV_ = (fV) % NSLOT; \
      while (FULL[sV_] < (unsigned)((fV) + 1)) __builtin_amdgcn_s_sleep(1); \
      CBAR(); \
      _Pragma("unroll") for (int d = 0; d < 8; ++d) vf[d] = __builtin_amdgcn_ds_read_tr16_b64_v4i16((LAS s16x4*)(L8 + sV_ * SLOT + vbase + d * 32)); \
      LDSW(); FREEC[sV_ * 4] = (unsigned)((fV) + 1); \
      asm volatile("" : "+v"(vf[0]), "+v"(vf[1]), "+v"(vf[2]), "+v"(vf[3]), "+v"(vf[4]), "+v"(vf[5]), "+v"(vf[6]), "+v"(vf[7]) :: "memory"); } while (0)
    auto qk_softmax = [&](const bf16x8 (&kf)[4]) __attribute__((always_inline)) -> s16x4 {
      f32x4 S0 = {0.f, 0.f, 0.f, 0.f}, S1 = {0.f, 0.f, 0.f, 0.f};
      S0 = __builtin_amdgcn_mfma_f32_16x16x32_bf16(kf[0], qf[0], S0, 0, 0, 0);
      S1 = __builtin_amdgcn_mfma_f32_16x16x32_bf16(kf[1], qf[1], S1, 0, 0, 0);
      S0 = __builtin_amdgcn_mfma_f32_16x16x32_bf16(kf[2], qf[2], S0, 0, 0, 0);
      S1 = __builtin_amdgcn_mfma_f32_16x16x32_bf16(kf[3], qf[3], S1, 0, 0, 0);
      float sc[4];
#pragma unroll
      for (int r = 0; r < 4; ++r) sc[r] = rbf(rbf(S0[r] + S1[r]) * SCALE);
      const float pm = xrow_max(fmaxf(fmaxf(sc[0], sc[1]), fmaxf(sc[2], sc[3])));
      if (__any(pm > m)) {
        const float mn = fmaxf(m, pm);
        const float alpha = __builtin_amdgcn_exp2f((m - mn) * L2E);
        m = mn; l *= alpha;
        float al[4];
#pragma unroll
        for (int r = 0; r < 4; ++r) al[r] = __builtin_bit_cast(float, __builtin_amdgcn_readlane(__builtin_bit_cast(int, alpha), r));
#pragma unroll
        for (int d = 0; d < 8; ++d)
#pragma unroll
          for (int r = 0; r < 4; ++r) O[d][r] *= al[r];
      }
      float p[4];
#pragma unroll
      for (int r = 0; r < 4; ++r) p[r] = __builtin_amdgcn_exp2f((sc[r] - m) * L2E);
      l += (p[0] + p[1]) + (p[2] + p[3]);
      return __builtin_bit_cast(s16x4, v2u{cvtpk(p[0], p[1]), cvtpk(p[2], p[3])});
    };
    auto pv = [&](const s16x4 pf, const s16x4 (&vf)[8]) __attribute__((always_inline)) {
#pragma unroll
      for (int d = 0; d < 8; ++d) O[d] = __builtin_amdgcn_mfma_f32_16x16x16bf16_1k(pf, vf[d], O[d], 0, 0, 0);
    };
    {
      bf16x8 k0[4], k1[4], k2[4], k3[4]; s16x4 v0[8], v1[8], v2[8], v3[8];
#define WAITK(f) do { while (FULL[(f) % NSLOT] < (unsigned)((f) + 1)) __builtin_amdgcn_s_sleep(1); CBAR(); } while (0)
      WAITK(0); READ_K(k0, 0); READ_V(v0, 1);
      WAITK(2); READ_K(k1, 2); READ_V(v1, 3);
      WAITK(4); READ_K(k2, 4); READ_V(v2, 5);
      WAITK(6); READ_K(k3, 6); READ_V(v3, 7);
      while (MISC[M_QRDY] == 0u) __builtin_amdgcn_s_sleep(1);
      CBAR();
#pragma unroll
      for (int ks = 0; ks < 4; ++ks) qf[ks] = *(const LAS bf16x8*)(L8 + L_QL + (n & 3) * 256 + ks * 64 + gq * 16);
      pv(qk_softmax(k0), v0); pv(qk_softmax(k1), v1); pv(qk_softmax(k2), v2); pv(qk_softmax(k3), v3);
    }
#pragma unroll 1
    for (int t = 4;; ++t) {
      const int fK = 2 * t, fV = 2 * t + 1;
      bool done = false;
      while (FULL[fK % NSLOT] < (unsigned)(fK + 1)) {
        const unsigned nt = MISC[M_NT];
        if (nt != 0u && (unsigned)t + 1u >= nt) { done = true; break; }
        __builtin_amdgcn_s_sleep(1);
      }
      if (done) break;
      CBAR();
      bf16x8 kf[4]; s16x4 vf[8];
      READ_K(kf, fK);
      const s16x4 pf = qk_softmax(kf);
      READ_V(vf, fV);
      pv(pf, vf);
    }
    STAMP(3);
    const float lt = xrow_sum(l);
    {
      LAS float* mrg = (LAS float*)(L8 + L_MRG + c * 2048 + gq * 512 + n * 4);
#pragma unroll
      for (int d = 0; d < 8; ++d) {
        const float lo = (gq & 1) ? O[d][1] : O[d][0], hi = (gq & 1) ? O[d][3] : O[d][2];
        mrg[d * 16] = (gq & 2) ? hi : lo;
      }
      if (lane < 4) *(LAS v2u*)(L8 + L_ML + lane * 32 + c * 8) = v2u{__float_as_uint(m), __float_as_uint(lt)};
    }
    LDSW();
    if (lane == 0) __hip_atomic_fetch_add((LAS unsigned*)(L8 + L_MISC + 4 * M_CCNT), 1u, __ATOMIC_RELAXED, __HIP_MEMORY_SCOPE_WORKGROUP);
    while (MISC[M_CCNT] < 4u) { }
    CBAR();
    {
      const f32x4 ml01 = *(const LAS f32x4*)(L8 + L_ML + c * 32), ml23 = *(const LAS f32x4*)(L8 + L_ML + c * 32 + 16);
      f32x4 ov[4];
#pragma unroll
      for (int w = 0; w < 4; ++w) ov[w] = *(const LAS f32x4*)(L8 + L_MRG + w * 2048 + c * 512 + (lane & 31) * 16);
      const float M = fmaxf(fmaxf(ml01[0], ml01[2]), fmaxf(ml23[0], ml23[2]));
      const float w0 = __builtin_amdgcn_exp2f((ml01[0] - M) * L2E), w1v = __builtin_amdgcn_exp2f((ml01[2] - M) * L2E);
      const float w2v = __builtin_amdgcn_exp2f((ml23[0] - M) * L2E), w3v = __builtin_amdgcn_exp2f((ml23[2] - M) * L2E);
      const f32x4 o = (w0 * ov[0] + w1v * ov[1]) + (w2v * ov[2] + w3v * ov[3]);
      const float L = (w0 * ml01[1] + w1v * ml01[3]) + (w2v * ml23[1] + w3v * ml23[3]);
      const size_t off = W_PART + ((size_t)(4 * g + c) * 32 + s) * PROW;
      const v4u val = lane < 32 ? sane4(o) : v4u{sane(M), sane(L), 0u, 0u};
      if (lane <= 32) ST16(val, off + lane * 16);
    }
    STAMP(4);
  } else if (wave == 5) {
    const u64 E = __hip_atomic_load((gu64*)(ws + W_E), RLX_AGENT);
    const v4u sv = {SENT, SENT, SENT, SENT};
#pragma unroll
    for (int h = 0; h < 4; ++h)
      if (lane < 33) ST16(sv, W_PART + ((size_t)(4 * g + h) * 32 + s) * PROW + lane * 16);
    if (lane < 16) ST16(sv, W_AP + ((size_t)hh * 512 + cb * 64) * 4 + lane * 16);
    else if (lane < 32) ST16(sv, W_BP + ((size_t)hh * 512 + cb * 64) * 4 + (lane - 16) * 16);
    else if (lane < 48 && s < 28) ST16(sv, W_GP + ((size_t)g * FF + s * 64) * 4 + (lane - 32) * 16);
    if (lane == 63 && s == 0) __hip_atomic_store((gu32*)(ws + W_CTR + g * 128), 0u, RLX_AGENT);
    LDSW(); __builtin_amdgcn_s_barrier();
    VMW0();
    const u64 tag = E + 1;
    if (lane == 0) { MISC[M_TAGLO] = (unsigned)tag; MISC[M_TAGHI] = (unsigned)(tag >> 32); }
    LDSW();
    if (lane == 0) MISC[M_CLR] = 1u;
    for (unsigned spins = 0; spins < SPIN_CAP; ++spins) {
      const u64 v = __hip_atomic_load(QF + 32 * g + (lane & 31), RLX_AGENT);
      if (__all(v == tag)) break;
      __builtin_amdgcn_s_sleep(2);
    }
    CBAR();
    f32x4 qa[4], qb[4];
#pragma unroll
    for (int r = 0; r < 4; ++r) {
      qa[r] = __builtin_bit_cast(f32x4, LD16(W_QPG + ((size_t)(g * 4 + r) * 512 + 8 * lane) * 4));
      qb[r] = __builtin_bit_cast(f32x4, LD16(W_QPG + ((size_t)(g * 4 + r) * 512 + 8 * lane + 4) * 4));
    }
    const f32x4 q0 = (qa[0] + qa[1]) + (qa[2] + qa[3]), q1 = (qb[0] + qb[1]) + (qb[2] + qb[3]);
    *(LAS v4u*)(L8 + L_QL + lane * 16) = v4u{cvtpk(q0[0], q0[1]), cvtpk(q0[2], q0[3]), cvtpk(q1[0], q1[1]), cvtpk(q1[2], q1[3])};
    LDSW();
    if (lane == 0) MISC[M_QRDY] = 1u;
    STAMP(2);
    for (int j = 0;; ++j) {
      while ((int)MISC[M_LDI] + DEPTH < NSTAT + j) __builtin_amdgcn_s_sleep(2);
      unsigned idx = 0;
      if (lane == 0) idx = __hip_atomic_fetch_add((gu32*)(ws + W_CTR + g * 128), 1u, RLX_AGENT);
      idx = __builtin_amdgcn_readfirstlane(idx);
      const unsigned tile = idx < (unsigned)NDYN ? (idx & 31u) * 32u + (unsigned)NSTAT + (idx >> 5) : TQ_END;
      if (lane == 0) ((volatile LAS unsigned*)(L8 + L_TQ))[j & 7] = tile;
      LDSW();
      if (lane == 0) MISC[M_TQN] = (unsigned)(j + 1);
      if (tile == TQ_END) break;
    }
  } else if (wave == 6) {
    LDSW(); __builtin_amdgcn_s_barrier();
    if (s < 28) {
      while (MISC[M_QRDY] == 0u) __builtin_amdgcn_s_sleep(4);
      CBAR();
      const u16* W = (g >= 4) ? w3 : w1;
      const int r0 = 128 * (g & 3) + (lane >> 3);
      v4u a[16];
#pragma unroll
      for (int i = 0; i < 16; ++i) a[i] = ldnt(W + (size_t)(r0 + 8 * i) * FF + 64 * s + 8 * (lane & 7));
      float acc[8] = {0.f, 0.f, 0.f, 0.f, 0.f, 0.f, 0.f, 0.f};
#pragma unroll
      for (int i = 0; i < 16; ++i) fma8(acc, bf2f(((const LAS u16*)(L8 + L_XN))[r0 + 8 * i]), a[i]);
#pragma unroll
      for (int k = 0; k < 8; ++k) { float t = acc[k]; t += dpp<ROR8>(t); acc[k] = xrow_sum(t); }
      if (lane < 8) {
        const size_t off = W_GP + ((size_t)g * FF + s * 64 + lane * 8) * 4;
        ST16(sane4(f32x4{acc[0], acc[1], acc[2], acc[3]}), off);
        ST16(sane4(f32x4{acc[4], acc[5], acc[6], acc[7]}), off + 16);
      }
    }
  } else {
    LDSW(); __builtin_amdgcn_s_barrier();
    while (MISC[M_QRDY] == 0u) __builtin_amdgcn_s_sleep(4);
    CBAR();
    const u64 tag = ((u64)MISC[M_TAGHI] << 32) | (u64)MISC[M_TAGLO];
    v4u wv[7];
#pragma unroll
    for (int i = 0; i < 7; ++i) wv[i] = ldnt(w2 + (size_t)(56 * hh + 8 * i + (lane >> 3)) * 512 + 64 * cb + 8 * (lane & 7));
    int pidx;
    if (lane < 32) { const int cq = (56 * hh + ((lane & 8) ? 55 : 0)) >> 6; pidx = 32 * (lane & 7) + cq; }
    else { const int h2 = lane - 32; pidx = 32 * (h2 >> 2) + 8 * (h2 & 3) + cb; }
    for (unsigned spins = 0; spins < SPIN_CAP; ++spins) {
      const u64 v = __hip_atomic_load(QF + pidx, RLX_AGENT);
      if (__all(v == tag)) break;
      __builtin_amdgcn_s_sleep(8);
    }
    CBAR();
    if (lane == 0) MISC[M_CHK] = 1u;
    unsigned gp[8];
    const int col = 56 * hh + (lane < 56 ? lane : 0);
    for (unsigned spins = 0; spins < SPIN_CAP; ++spins) {
      CBAR();
      bool ok = true;
#pragma unroll
      for (int r = 0; r < 8; ++r) { gp[r] = LD4(W_GP + ((size_t)r * FF + col) * 4); ok &= gp[r] != SENT; }
      if (__all(ok)) break;
      __builtin_amdgcn_s_sleep(16);
    }
    const float gb = rbf((__uint_as_float(gp[0]) + __uint_as_float(gp[1])) + (__uint_as_float(gp[2]) + __uint_as_float(gp[3])));
    const float ub = rbf((__uint_as_float(gp[4]) + __uint_as_float(gp[5])) + (__uint_as_float(gp[6]) + __uint_as_float(gp[7])));
    const float sg = rbf(gb / (1.f + __expf(-gb)));
    const float hv = rbf(sg * ub);
    float acc[8] = {0.f, 0.f, 0.f, 0.f, 0.f, 0.f, 0.f, 0.f};
#pragma unroll
    for (int i = 0; i < 7; ++i) fma8(acc, __shfl(hv, 8 * i + (lane >> 3)), wv[i]);
#pragma unroll
    for (int k = 0; k < 8; ++k) { float t = acc[k]; t += dpp<ROR8>(t); acc[k] = xrow_sum(t); }
    if (lane < 8) {
      const size_t off = W_BP + ((size_t)hh * 512 + cb * 64 + lane * 8) * 4;
      ST16(sane4(f32x4{acc[0], acc[1], acc[2], acc[3]}), off);
      ST16(sane4(f32x4{acc[4], acc[5], acc[6], acc[7]}), off + 16);
    }
  }

  while (MISC[M_CCNT] < 4u) __builtin_amdgcn_s_sleep(2);
  CBAR();
  const int wslot = __builtin_amdgcn_readfirstlane((int)MISC[M_WOS]);
  const int L_WO = wslot * SLOT;
  const int L_RED = (wslot >= 4 ? wslot - 4 : wslot + 4) * SLOT;
  const int L_RML = L_RED + 4096, L_RED2 = L_RED + 4096 + 64;
  {
    const int c4 = tid & 31, sg = tid >> 5;
    const size_t pbase = W_PART + (size_t)hh * 32 * PROW;
    v4u o0 = {SENT, SENT, SENT, SENT}, o1 = {SENT, SENT, SENT, SENT}; v2u ml0 = {SENT, SENT}, ml1 = {SENT, SENT};
    STAMP(8);
    for (unsigned spins = 0; spins < SPIN_CAP; ++spins) {
      CBAR();
      if (!valid4(o0) || !valid2(ml0)) { o0 = LD16(pbase + sg * PROW + c4 * 16); ml0 = LD8(pbase + sg * PROW + 512); }
      if (!valid4(o1) || !valid2(ml1)) { o1 = LD16(pbase + (sg + 16) * PROW + c4 * 16); ml1 = LD8(pbase + (sg + 16) * PROW + 512); }
      if (__all(valid4(o0) && valid4(o1) && valid2(ml0) && valid2(ml1))) break;
      __builtin_amdgcn_s_sleep(1);
    }
    STAMP(5);
    const float m0 = __uint_as_float(ml0[0]), m1 = __uint_as_float(ml1[0]);
    const float Mw = x32_max(fmaxf(m0, m1));
    const float e0 = __builtin_amdgcn_exp2f((m0 - Mw) * L2E), e1 = __builtin_amdgcn_exp2f((m1 - Mw) * L2E);
    const float Lw = x32_sum(fmaf(e0, __uint_as_float(ml0[1]), e1 * __uint_as_float(ml1[1])));
    f32x4 o;
#pragma unroll
    for (int k = 0; k < 4; ++k) o[k] = x32_sum(fmaf(e0, __uint_as_float(o0[k]), e1 * __uint_as_float(o1[k])));
    if (lane < 32) *(LAS f32x4*)(L8 + L_RED + wave * 512 + c4 * 16) = o;
    if (lane == 0) *(LAS v2u*)(L8 + L_RML + wave * 8) = v2u{__float_as_uint(Mw), __float_as_uint(Lw)};
    STAMP(9);
    __syncthreads();
    STAMP(12);
  }
  {
    const int j = lane >> 4, p = lane & 15, c2 = lane & 31, hf = lane >> 5;
    const f32x4 mlj = *(const LAS f32x4*)(L8 + L_RML + j * 16);
    const float r0 = *(const LAS float*)(L8 + L_RED + (2 * j) * 512 + (16 * wave + p) * 4);
    const float r1 = *(const LAS float*)(L8 + L_RED + (2 * j + 1) * 512 + (16 * wave + p) * 4);
    unsigned ww[8];
#pragma unroll
    for (int i = 0; i < 8; ++i) ww[i] = *(const LAS unsigned*)(L8 + L_WO + (2 * wave + hf) * BLK + i * 128 + c2 * 4);
    const float M = xrow_max(fmaxf(mlj[0], mlj[2]));
    const float f0 = __builtin_amdgcn_exp2f((mlj[0] - M) * L2E), f1 = __builtin_amdgcn_exp2f((mlj[2] - M) * L2E);
    const float L = xrow_sum(fmaf(f0, mlj[1], f1 * mlj[3]));
    const float ov = rbf(xrow_sum(fmaf(f0, r0, f1 * r1)) / L);
    const float ovr = dpp<ROR8>(ov);
    const float o2 = hf ? ovr : ov;
    float a0 = 0.f, a1 = 0.f;
#define WOSTEP(i) { const float oi = dpp<NEWBCAST + (i)>(o2); a0 = fmaf(oi, bf2f(ww[i] & 0xffffu), a0); a1 = fmaf(oi, bfhi(ww[i]), a1); }
    WOSTEP(0) WOSTEP(1) WOSTEP(2) WOSTEP(3) WOSTEP(4) WOSTEP(5) WOSTEP(6) WOSTEP(7)
#undef WOSTEP
    a0 = x32_sum(a0); a1 = x32_sum(a1);
    if (lane < 32) *(LAS v2u*)(L8 + L_RED2 + wave * 256 + c2 * 8) = v2u{__float_as_uint(a0), __float_as_uint(a1)};
    STAMP(13);
    __syncthreads();
  }
  if (wave == 0) {
    {
      f32x4 a[8];
#pragma unroll
      for (int w = 0; w < 8; ++w) a[w] = *(const LAS f32x4*)(L8 + L_RED2 + w * 256 + (lane & 15) * 16);
      const f32x4 t = ((a[0] + a[1]) + (a[2] + a[3])) + ((a[4] + a[5]) + (a[6] + a[7]));
      if (lane < 16) ST16(sane4(t), W_AP + ((size_t)hh * 512 + cb * 64) * 4 + lane * 16);
    }
    STAMP(6);
    while (MISC[M_CHK] == 0u) __builtin_amdgcn_s_sleep(1);
    CBAR();
    const int col = 64 * cb + 2 * hh;
    const unsigned xw = *(const GAS unsigned*)(x + col);
    const size_t off = (lane < 32 ? W_AP : W_BP) + ((size_t)(lane & 31) * 512 + col) * 4;
    v2u v = {SENT, SENT};
    for (unsigned spins = 0; spins < SPIN_CAP; ++spins) {
      CBAR();
      if (!valid2(v)) v = LD8(off);
      if (__all(valid2(v))) break;
      __builtin_amdgcn_s_sleep(1);
    }
    STAMP(7);
    float r0 = __uint_as_float(v[0]), r1 = __uint_as_float(v[1]);
    r0 += dpp<ROR1>(r0); r0 += dpp<ROR2>(r0); r0 += dpp<ROR4>(r0); r0 += dpp<ROR8>(r0); r0 = x16_sum(r0);
    r1 += dpp<ROR1>(r1); r1 += dpp<ROR2>(r1); r1 += dpp<ROR4>(r1); r1 += dpp<ROR8>(r1); r1 = x16_sum(r1);
    const float B0 = __shfl(r0, 32), B1 = __shfl(r1, 32);
    const float y0 = rbf(bf2f(xw & 0xffffu) + rbf(r0)) + rbf(B0);
    const float y1 = rbf(bfhi(xw) + rbf(r1)) + rbf(B1);
    if (lane == 0) {
      *(GAS unsigned*)(out + col) = cvtpk(y0, y1);
      if (b == 0) {
        const u64 tag = ((u64)MISC[M_TAGHI] << 32) | (u64)MISC[M_TAGLO];
        __hip_atomic_store((gu64*)(ws + W_E), tag, RLX_AGENT);
      }
    }
  }
}

extern "C" void kernel_launch(void* const* d_in, const int* in_sizes, int n_in,
                              void* d_out, int out_size, void* d_ws, size_t ws_size,
                              hipStream_t stream) {
  fused_kernel<<<256, 512, 0, stream>>>((const u16*)d_in[0], (const u16*)d_in[1], (const u16*)d_in[2], (const u16*)d_in[3], (const u16*)d_in[4],
                                        (const u16*)d_in[6], (const u16*)d_in[7], (const u16*)d_in[8], (u16*)d_out, (unsigned char*)d_ws);
}
```

```cpp
#include <hip/hip_runtime.h>
#include <hip/hip_bf16.h>
#include <cstdint>

#define GAS __attribute__((address_space(1)))
#define LAS __attribute__((address_space(3)))
typedef unsigned short u16;
typedef unsigned long long u64;
typedef unsigned v4u __attribute__((ext_vector_type(4)));
typedef unsigned v2u __attribute__((ext_vector_type(2)));
typedef float f32x4 __attribute__((ext_vector_type(4)));
typedef __bf16 bf16x8 __attribute__((ext_vector_type(8)));
typedef short s16x4 __attribute__((ext_vector_type(4)));
typedef GAS unsigned gu32;
typedef GAS u64 gu64;
#define RLX_AGENT __ATOMIC_RELAXED, __HIP_MEMORY_SCOPE_AGENT

constexpr int SK = 65536, FF = 1792;
constexpr int BLK = 1056, SLOT = 16 * BLK, NSLOT = 9;
constexpr int NTILE = 32, NFILL = 2 * NTILE + 1;
constexpr int L_CTRL = NSLOT * SLOT;
constexpr int L_FULL = L_CTRL;
constexpr int L_MISC = L_CTRL + 64;
constexpr int L_FREE = L_CTRL + 160;
constexpr int L_ML = L_CTRL + 320;
constexpr int L_QP = L_CTRL + 512;
constexpr int L_QL = L_QP + 1024;
constexpr int L_XN = L_QL + 1024;
constexpr int L_MRG = L_XN + 1024;
constexpr int L_TOTAL = L_MRG + 8192;
static_assert(L_TOTAL <= 163840, "lds");
constexpr int M_CLR = 0, M_QCNT = 1, M_QRDY = 2, M_TAGLO = 4, M_TAGHI = 5, M_CHK = 6, M_CCNT = 10;
constexpr int L_WO = ((NFILL - 1) % NSLOT) * SLOT;
constexpr int L_RED = (((NFILL - 1) + 4) % NSLOT) * SLOT;
constexpr int L_RML = L_RED + 4096, L_RED2 = L_RED + 4096 + 64;
constexpr size_t W_E = 0;
constexpr size_t W_QF = 4096;
constexpr size_t W_QPG = 8192;
constexpr size_t W_GP = W_QPG + 65536;
constexpr size_t W_BP = W_GP + 65536;
constexpr size_t W_AP = W_BP + 65536;
constexpr int PROW = 640;
constexpr size_t W_PART = W_AP + 65536;
constexpr size_t W_END = W_PART + (size_t)32 * 32 * PROW;
constexpr size_t W_STAMP = 4u << 20;
#define STAMP(k) do { } while (0)
constexpr unsigned SENT = 0xFFFFFFFFu;
constexpr unsigned SPIN_CAP = 1u << 20;

__device__ __forceinline__ float bf2f(unsigned b) { return __builtin_bit_cast(float, b << 16); }
__device__ __forceinline__ float bfhi(unsigned w) { return __builtin_bit_cast(float, w & 0xffff0000u); }
__device__ __forceinline__ unsigned cvtpk(float lo, float hi) {
  typedef __bf16 bf16x2 __attribute__((ext_vector_type(2)));
  typedef float f32x2 __attribute__((ext_vector_type(2)));
  f32x2 v = {lo, hi};
  return __builtin_bit_cast(unsigned, __builtin_convertvector(v, bf16x2));
}
__device__ __forceinline__ float rbf(float f) { return bf2f(cvtpk(f, 0.f) & 0xffffu); }
__device__ __forceinline__ unsigned sane(float f) { const unsigned u = __builtin_bit_cast(unsigned, f); return u == SENT ? 0x7FC00000u : u; }
__device__ __forceinline__ v4u sane4(f32x4 a) { return v4u{sane(a[0]), sane(a[1]), sane(a[2]), sane(a[3])}; }
__device__ __forceinline__ bool valid4(v4u a) { return a[0] != SENT && a[1] != SENT && a[2] != SENT && a[3] != SENT; }
__device__ __forceinline__ bool valid2(v2u a) { return a[0] != SENT && a[1] != SENT; }
template <int CTRL> __device__ __forceinline__ float dpp(float x) {
  return __builtin_bit_cast(float, __builtin_amdgcn_mov_dpp(__builtin_bit_cast(int, x), CTRL, 0xf, 0xf, true));
}
constexpr int ROR1 = 0x121, ROR2 = 0x122, ROR4 = 0x124, ROR8 = 0x128, NEWBCAST = 0x150;
__device__ __forceinline__ float x32_sum(float x) {
  auto t = __builtin_amdgcn_permlane32_swap(__float_as_uint(x), __float_as_uint(x), false, false);
  return __uint_as_float(t[0]) + __uint_as_float(t[1]);
}
__device__ __forceinline__ float x16_sum(float x) {
  auto t = __builtin_amdgcn_permlane16_swap(__float_as_uint(x), __float_as_uint(x), false, false);
  return __uint_as_float(t[0]) + __uint_as_float(t[1]);
}
__device__ __forceinline__ float x32_max(float x) {
  auto t = __builtin_amdgcn_permlane32_swap(__float_as_uint(x), __float_as_uint(x), false, false);
  return fmaxf(__uint_as_float(t[0]), __uint_as_float(t[1]));
}
__device__ __forceinline__ float x16_max(float x) {
  auto t = __builtin_amdgcn_permlane16_swap(__float_as_uint(x), __float_as_uint(x), false, false);
  return fmaxf(__uint_as_float(t[0]), __uint_as_float(t[1]));
}
__device__ __forceinline__ float xrow_max(float x) { return x32_max(x16_max(x)); }
__device__ __forceinline__ float xrow_sum(float x) { return x32_sum(x16_sum(x)); }
__device__ __forceinline__ float wave_sum(float x) {
  x += dpp<ROR1>(x); x += dpp<ROR2>(x); x += dpp<ROR4>(x); x += dpp<ROR8>(x);
  return xrow_sum(x);
}
__device__ __forceinline__ v4u ldnt(const void* p) { return __builtin_nontemporal_load((const GAS v4u*)p); }
#define LDSW() asm volatile("s_waitcnt lgkmcnt(0)" ::: "memory")
#define VMW0() asm volatile("s_waitcnt vmcnt(0)" ::: "memory")
#define CBAR() asm volatile("" ::: "memory")
#define GLDS(src, dst) __builtin_amdgcn_global_load_lds((const unsigned*)(src), (unsigned*)(dst), 16, 0, 2)
#define ST16(v, off) __builtin_amdgcn_raw_buffer_store_b128((v), rs, (int)(off), 0, 16)
#define LD16(off) __builtin_amdgcn_raw_buffer_load_b128(rs, (int)(off), 0, 16)
#define LD8(off) __builtin_amdgcn_raw_buffer_load_b64(rs, (int)(off), 0, 16)
#define LD4(off) __builtin_amdgcn_raw_buffer_load_b32(rs, (int)(off), 0, 16)

__device__ __forceinline__ void fma8(float (&acc)[8], float xv, v4u w) {
#pragma unroll
  for (int k = 0; k < 4; ++k) { acc[2 * k] = fmaf(xv, bf2f(w[k] & 0xffffu), acc[2 * k]); acc[2 * k + 1] = fmaf(xv, bfhi(w[k]), acc[2 * k + 1]); }
}

__global__ void __launch_bounds__(512, 2) fused_kernel(const u16* __restrict__ x, const u16* __restrict__ kc, const u16* __restrict__ vc,
                                                       const u16* __restrict__ wq, const u16* __restrict__ wo, const u16* __restrict__ w1,
                                                       const u16* __restrict__ w3, const u16* __restrict__ w2, u16* __restrict__ out,
                                                       unsigned char* ws) {
  __shared__ __attribute__((aligned(16))) unsigned char lds[L_TOTAL];
  LAS unsigned char* L8 = (LAS unsigned char*)lds;
  volatile LAS unsigned* FULL = (volatile LAS unsigned*)(L8 + L_FULL);
  volatile LAS unsigned* MISC = (volatile LAS unsigned*)(L8 + L_MISC);
  const int tid = threadIdx.x, lane = tid & 63, wave = __builtin_amdgcn_readfirstlane(tid >> 6);
#define K_OPT1 1
#define K_OPT2 0
  const int b = blockIdx.x, g = K_OPT1 ? (b >> 5) : (b & 7), s = K_OPT1 ? (b & 31) : (b >> 3);
  const int hh = 4 * g + (s >> 3), cb = s & 7;
  gu64* QF = (gu64*)(ws + W_QF);
  const __amdgpu_buffer_rsrc_t rs = __builtin_amdgcn_make_buffer_rsrc(ws, 0, (int)W_END, 0x00020000);
  constexpr float SCALE = 0.08838834764831845f, L2E = 1.4426950408889634f;

  if (wave == 1) { ((LAS unsigned*)(L8 + L_CTRL))[lane] = 0u; if (lane < 16) ((LAS unsigned*)(L8 + L_CTRL))[64 + lane] = 0u; }

  if (wave == 0) {
    const size_t kvoff = ((size_t)g * SK + (size_t)s * (NTILE * 64)) * 256;
    const unsigned char* kp = (const unsigned char*)kc + kvoff + lane * 16;
    const unsigned char* vp = (const unsigned char*)vc + kvoff + lane * 16;
    const unsigned char* wop = (const unsigned char*)wo + ((size_t)(128 * hh + (lane >> 3)) * 512 + cb * 64) * 2 + (lane & 7) * 16;
    int pub = 0, slot = 0, pslot = 0;
    LDSW(); __builtin_amdgcn_s_barrier();
#define FREE_MIN(sl) ({ const v4u fr_ = *(volatile LAS v4u*)(L8 + L_FREE + (sl) * 16); min(min(fr_[0], fr_[1]), min(fr_[2], fr_[3])); })
    for (int f = 0; f < NFILL; ++f) {
      const bool last = f == NFILL - 1;
      if (f >= NSLOT) {
        const unsigned need = (unsigned)(f - NSLOT + 1);
        if (FREE_MIN(slot) < need) {
          VMW0();
          for (; pub < f; ++pub) { FULL[pslot] = (unsigned)(pub + 1); pslot = pslot == NSLOT - 1 ? 0 : pslot + 1; }
          while (FREE_MIN(slot) < need) __builtin_amdgcn_s_sleep(1);
        }
        CBAR();
      }
      const unsigned char* src = last ? wop : (((f & 1) ? vp : kp) + (size_t)(f >> 1) * 16384);
      const int sstr = last ? 8192 : 1024;
      unsigned char* dst = lds + slot * SLOT;
#pragma unroll
      for (int j = 0; j < 16; ++j) GLDS(src + (size_t)j * sstr, dst + j * BLK);
      slot = slot == NSLOT - 1 ? 0 : slot + 1;
      if (f + 1 - pub > 2) {
        asm volatile("s_waitcnt vmcnt(32)" ::: "memory");
        FULL[pslot] = (unsigned)(pub + 1); ++pub; pslot = pslot == NSLOT - 1 ? 0 : pslot + 1;
      }
    }
    if (pub + 2 == NFILL) {
      asm volatile("s_waitcnt vmcnt(16)" ::: "memory");
      FULL[pslot] = (unsigned)(pub + 1);
    }
    STAMP(3);
  } else if (wave <= 4) {
    const int c = wave - 1;
    {
      const v4u xs = *(const GAS v4u*)(x + 8 * lane);
      v4u wv[4];
      const int r0 = 128 * (s >> 3) + 32 * c + (lane >> 3);
#pragma unroll
      for (int i = 0; i < 4; ++i) wv[i] = ldnt(wq + (size_t)(r0 + 8 * i) * 4096 + 512 * g + 64 * cb + 8 * (lane & 7));
      LDSW(); __builtin_amdgcn_s_barrier();
      float xv[8];
#pragma unroll
      for (int k = 0; k < 4; ++k) { xv[2 * k] = bf2f(xs[k] & 0xffffu); xv[2 * k + 1] = bfhi(xs[k]); }
      float sm = 0.f;
#pragma unroll
      for (int i = 0; i < 8; ++i) sm += xv[i];
      const float mean = wave_sum(sm) * (1.f / 512.f);
      float vv = 0.f;
#pragma unroll
      for (int i = 0; i < 8; ++i) { const float d = xv[i] - mean; vv = fmaf(d, d, vv); }
      const float var = wave_sum(vv) * (1.f / 512.f);
      const float mb = rbf(mean), den = rbf(__builtin_sqrtf(rbf(rbf(var) + 1e-5f)));
      v4u xn;
#pragma unroll
      for (int k = 0; k < 4; ++k) xn[k] = cvtpk(rbf(xv[2 * k] - mb) / den, rbf(xv[2 * k + 1] - mb) / den);
      *(LAS v4u*)(L8 + L_XN + lane * 16) = xn;
      LDSW();
      float acc[8] = {0.f, 0.f, 0.f, 0.f, 0.f, 0.f, 0.f, 0.f};
#pragma unroll
      for (int i = 0; i < 4; ++i) fma8(acc, bf2f(((const LAS u16*)(L8 + L_XN))[r0 + 8 * i]), wv[i]);
#pragma unroll
      for (int k = 0; k < 8; ++k) { float t = acc[k]; t += dpp<ROR8>(t); acc[k] = xrow_sum(t); }
      if (lane < 8) {
        LAS f32x4* qp = (LAS f32x4*)(L8 + L_QP + c * 256 + lane * 32);
        qp[0] = f32x4{acc[0], acc[1], acc[2], acc[3]}; qp[1] = f32x4{acc[4], acc[5], acc[6], acc[7]};
      }
      LDSW();
      unsigned old = 0;
      if (lane == 0) old = __hip_atomic_fetch_add((LAS unsigned*)(L8 + L_MISC + 4 * M_QCNT), 1u, __ATOMIC_RELAXED, __HIP_MEMORY_SCOPE_WORKGROUP);
      old = __builtin_amdgcn_readfirstlane(old);
      CBAR();
      if (old == 3u) {
        if (lane < 16) {
          const LAS f32x4* qp = (const LAS f32x4*)(L8 + L_QP + lane * 16);
          const f32x4 qv = (qp[0] + qp[16]) + (qp[32] + qp[48]);
          ST16(__builtin_bit_cast(v4u, qv), W_QPG + ((size_t)(g * 4 + (s >> 3)) * 512 + 64 * cb + 4 * lane) * 4);
        }
        VMW0();
        while (MISC[M_CLR] == 0u) __builtin_amdgcn_s_sleep(1);
        CBAR();
        const u64 tag = ((u64)MISC[M_TAGHI] << 32) | (u64)MISC[M_TAGLO];
        if (lane == 0) __hip_atomic_store(QF + 32 * g + s, tag, RLX_AGENT);
        STAMP(1);
      }
    }
    const int n = lane & 15, gq = lane >> 4;
    const int kbase = n * BLK + c * 256 + gq * 16;
    const int vbase = (lane >> 2) * BLK + c * 256 + (lane & 3) * 8;
    volatile LAS unsigned* FREEC = (volatile LAS unsigned*)(L8 + L_FREE + c * 4);
    bf16x8 qf[4];
    f32x4 O[8];
#pragma unroll
    for (int d = 0; d < 8; ++d) O[d] = f32x4{0.f, 0.f, 0.f, 0.f};
    float m = -INFINITY, l = 0.f;
#define READ_K(kf, fK) do { const int sK_ = (fK) % NSLOT; \
      _Pragma("unroll") for (int ks = 0; ks < 4; ++ks) kf[ks] = *(const LAS bf16x8*)(L8 + sK_ * SLOT + kbase + ks * 64); \
      LDSW(); FREEC[sK_ * 4] = (unsigned)((fK) + 1); \
      asm volatile("" : "+v"(kf[0]), "+v"(kf[1]), "+v"(kf[2]), "+v"(kf[3]) :: "memory"); } while (0)
#define READ_V(vf, fV) do { const int sV_ = (fV) % NSLOT; \
      while (FULL[sV_] < (unsigned)((fV) + 1)) { if (K_OPT2) __builtin_amdgcn_s_sleep(K_OPT2); } \
      CBAR(); \
      _Pragma("unroll") for (int d = 0; d < 8; ++d) vf[d] = __builtin_amdgcn_ds_read_tr16_b64_v4i16((LAS s16x4*)(L8 + sV_ * SLOT + vbase + d * 32)); \
      LDSW(); FREEC[sV_ * 4] = (unsigned)((fV) + 1); \
      asm volatile("" : "+v"(vf[0]), "+v"(vf[1]), "+v"(vf[2]), "+v"(vf[3]), "+v"(vf[4]), "+v"(vf[5]), "+v"(vf[6]), "+v"(vf[7]) :: "memory"); } while (0)
    auto qk_softmax = [&](const bf16x8 (&kf)[4]) __attribute__((always_inline)) -> s16x4 {
      f32x4 S0 = {0.f, 0.f, 0.f, 0.f}, S1 = {0.f, 0.f, 0.f, 0.f};
      S0 = __builtin_amdgcn_mfma_f32_16x16x32_bf16(kf[0], qf[0], S0, 0, 0, 0);
      S1 = __builtin_amdgcn_mfma_f32_16x16x32_bf16(kf[1], qf[1], S1, 0, 0, 0);
      S0 = __builtin_amdgcn_mfma_f32_16x16x32_bf16(kf[2], qf[2], S0, 0, 0, 0);
      S1 = __builtin_amdgcn_mfma_f32_16x16x32_bf16(kf[3], qf[3], S1, 0, 0, 0);
      float sc[4];
#pragma unroll
      for (int r = 0; r < 4; ++r) sc[r] = rbf(rbf(S0[r] + S1[r]) * SCALE);
      const float pm = xrow_max(fmaxf(fmaxf(sc[0], sc[1]), fmaxf(sc[2], sc[3])));
      if (__any(pm > m)) {
        const float mn = fmaxf(m, pm);
        const float alpha = __builtin_amdgcn_exp2f((m - mn) * L2E);
        m = mn; l *= alpha;
        float al[4];
#pragma unroll
        for (int r = 0; r < 4; ++r) al[r] = __builtin_bit_cast(float, __builtin_amdgcn_readlane(__builtin_bit_cast(int, alpha), r));
#pragma unroll
        for (int d = 0; d < 8; ++d)
#pragma unroll
          for (int r = 0; r < 4; ++r) O[d][r] *= al[r];
      }
      float p[4];
#pragma unroll
      for (int r = 0; r < 4; ++r) p[r] = __builtin_amdgcn_exp2f((sc[r] - m) * L2E);
      l += (p[0] + p[1]) + (p[2] + p[3]);
      return __builtin_bit_cast(s16x4, v2u{cvtpk(p[0], p[1]), cvtpk(p[2], p[3])});
    };
    auto pv = [&](const s16x4 pf, const s16x4 (&vf)[8]) __attribute__((always_inline)) {
#pragma unroll
      for (int d = 0; d < 8; ++d) O[d] = __builtin_amdgcn_mfma_f32_16x16x16bf16_1k(pf, vf[d], O[d], 0, 0, 0);
    };
    {
      bf16x8 k0[4], k1[4], k2[4], k3[4]; s16x4 v0[8], v1[8], v2[8], v3[8];
#define WAITK(f) do { while (FULL[(f) % NSLOT] < (unsigned)((f) + 1)) __builtin_amdgcn_s_sleep(1); CBAR(); } while (0)
      WAITK(0); READ_K(k0, 0); READ_V(v0, 1);
      WAITK(2); READ_K(k1, 2); READ_V(v1, 3);
      WAITK(4); READ_K(k2, 4); READ_V(v2, 5);
      WAITK(6); READ_K(k3, 6); READ_V(v3, 7);
      while (MISC[M_QRDY] == 0u) __builtin_amdgcn_s_sleep(1);
      CBAR();
#pragma unroll
      for (int ks = 0; ks < 4; ++ks) qf[ks] = *(const LAS bf16x8*)(L8 + L_QL + (n & 3) * 256 + ks * 64 + gq * 16);
      pv(qk_softmax(k0), v0); pv(qk_softmax(k1), v1); pv(qk_softmax(k2), v2); pv(qk_softmax(k3), v3);
    }
#pragma unroll 1
    for (int t = 4; t < NTILE; ++t) {
      const int fK = 2 * t, fV = 2 * t + 1;
      while (FULL[fK % NSLOT] < (unsigned)(fK + 1)) { if (K_OPT2) __builtin_amdgcn_s_sleep(K_OPT2); }
      CBAR();
      bf16x8 kf[4]; s16x4 vf[8];
      READ_K(kf, fK);
      const s16x4 pf = qk_softmax(kf);
      READ_V(vf, fV);
      pv(pf, vf);
    }
    STAMP(3);
    const float lt = xrow_sum(l);
    {
      LAS float* mrg = (LAS float*)(L8 + L_MRG + c * 2048 + gq * 512 + n * 4);
#pragma unroll
      for (int d = 0; d < 8; ++d) {
        const float lo = (gq & 1) ? O[d][1] : O[d][0], hi = (gq & 1) ? O[d][3] : O[d][2];
        mrg[d * 16] = (gq & 2) ? hi : lo;
      }
      if (lane < 4) *(LAS v2u*)(L8 + L_ML + lane * 32 + c * 8) = v2u{__float_as_uint(m), __float_as_uint(lt)};
    }
    LDSW();
    if (lane == 0) __hip_atomic_fetch_add((LAS unsigned*)(L8 + L_MISC + 4 * M_CCNT), 1u, __ATOMIC_RELAXED, __HIP_MEMORY_SCOPE_WORKGROUP);
    while (MISC[M_CCNT] < 4u) { }
    CBAR();
    {
      const f32x4 ml01 = *(const LAS f32x4*)(L8 + L_ML + c * 32), ml23 = *(const LAS f32x4*)(L8 + L_ML + c * 32 + 16);
      f32x4 ov[4];
#pragma unroll
      for (int w = 0; w < 4; ++w) ov[w] = *(const LAS f32x4*)(L8 + L_MRG + w * 2048 + c * 512 + (lane & 31) * 16);
      const float M = fmaxf(fmaxf(ml01[0], ml01[2]), fmaxf(ml23[0], ml23[2]));
      const float w0 = __builtin_amdgcn_exp2f((ml01[0] - M) * L2E), w1v = __builtin_amdgcn_exp2f((ml01[2] - M) * L2E);
      const float w2v = __builtin_amdgcn_exp2f((ml23[0] - M) * L2E), w3v = __builtin_amdgcn_exp2f((ml23[2] - M) * L2E);
      const f32x4 o = (w0 * ov[0] + w1v * ov[1]) + (w2v * ov[2] + w3v * ov[3]);
      const float L = (w0 * ml01[1] + w1v * ml01[3]) + (w2v * ml23[1] + w3v * ml23[3]);
      const size_t off = W_PART + ((size_t)(4 * g + c) * 32 + s) * PROW;
      const v4u val = lane < 32 ? sane4(o) : v4u{sane(M), sane(L), 0u, 0u};
      if (lane <= 32) ST16(val, off + lane * 16);
    }
    STAMP(4);
  } else if (wave == 5) {
    const u64 E = __hip_atomic_load((gu64*)(ws + W_E), RLX_AGENT);
    const v4u sv = {SENT, SENT, SENT, SENT};
#pragma unroll
    for (int h = 0; h < 4; ++h)
      if (lane < 33) ST16(sv, W_PART + ((size_t)(4 * g + h) * 32 + s) * PROW + lane * 16);
    if (lane < 16) ST16(sv, W_AP + ((size_t)hh * 512 + cb * 64) * 4 + lane * 16);
    else if (lane < 32) ST16(sv, W_BP + ((size_t)hh * 512 + cb * 64) * 4 + (lane - 16) * 16);
    else if (lane < 48 && s < 28) ST16(sv, W_GP + ((size_t)g * FF + s * 64) * 4 + (lane - 32) * 16);
    LDSW(); __builtin_amdgcn_s_barrier();
    VMW0();
    const u64 tag = E + 1;
    if (lane == 0) { MISC[M_TAGLO] = (unsigned)tag; MISC[M_TAGHI] = (unsigned)(tag >> 32); }
    LDSW();
    if (lane == 0) MISC[M_CLR] = 1u;
    for (unsigned spins = 0; spins < SPIN_CAP; ++spins) {
      const u64 v = __hip_atomic_load(QF + 32 * g + (lane & 31), RLX_AGENT);
      if (__all(v == tag)) break;
      __builtin_amdgcn_s_sleep(2);
    }
    CBAR();
    f32x4 qa[4], qb[4];
#pragma unroll
    for (int r = 0; r < 4; ++r) {
      qa[r] = __builtin_bit_cast(f32x4, LD16(W_QPG + ((size_t)(g * 4 + r) * 512 + 8 * lane) * 4));
      qb[r] = __builtin_bit_cast(f32x4, LD16(W_QPG + ((size_t)(g * 4 + r) * 512 + 8 * lane + 4) * 4));
    }
    const f32x4 q0 = (qa[0] + qa[1]) + (qa[2] + qa[3]), q1 = (qb[0] + qb[1]) + (qb[2] + qb[3]);
    *(LAS v4u*)(L8 + L_QL + lane * 16) = v4u{cvtpk(q0[0], q0[1]), cvtpk(q0[2], q0[3]), cvtpk(q1[0], q1[1]), cvtpk(q1[2], q1[3])};
    LDSW();
    if (lane == 0) MISC[M_QRDY] = 1u;
    STAMP(2);
  } else if (wave == 6) {
    LDSW(); __builtin_amdgcn_s_barrier();
    if (s < 28) {
      while (MISC[M_QRDY] == 0u) __builtin_amdgcn_s_sleep(4);
      CBAR();
      const u16* W = (g >= 4) ? w3 : w1;
      const int r0 = 128 * (g & 3) + (lane >> 3);
      v4u a[16];
#pragma unroll
      for (int i = 0; i < 16; ++i) a[i] = ldnt(W + (size_t)(r0 + 8 * i) * FF + 64 * s + 8 * (lane & 7));
      float acc[8] = {0.f, 0.f, 0.f, 0.f, 0.f, 0.f, 0.f, 0.f};
#pragma unroll
      for (int i = 0; i < 16; ++i) fma8(acc, bf2f(((const LAS u16*)(L8 + L_XN))[r0 + 8 * i]), a[i]);
#pragma unroll
      for (int k = 0; k < 8; ++k) { float t = acc[k]; t += dpp<ROR8>(t); acc[k] = xrow_sum(t); }
      if (lane < 8) {
        const size_t off = W_GP + ((size_t)g * FF + s * 64 + lane * 8) * 4;
        ST16(sane4(f32x4{acc[0], acc[1], acc[2], acc[3]}), off);
        ST16(sane4(f32x4{acc[4], acc[5], acc[6], acc[7]}), off + 16);
      }
    }
  } else {
    LDSW(); __builtin_amdgcn_s_barrier();
    while (MISC[M_QRDY] == 0u) __builtin_amdgcn_s_sleep(4);
    CBAR();
    const u64 tag = ((u64)MISC[M_TAGHI] << 32) | (u64)MISC[M_TAGLO];
    v4u wv[7];
#pragma unroll
    for (int i = 0; i < 7; ++i) wv[i] = ldnt(w2 + (size_t)(56 * hh + 8 * i + (lane >> 3)) * 512 + 64 * cb + 8 * (lane & 7));
    int pidx;
    if (lane < 32) { const int cq = (56 * hh + ((lane & 8) ? 55 : 0)) >> 6; pidx = 32 * (lane & 7) + cq; }
    else { const int h2 = lane - 32; pidx = 32 * (h2 >> 2) + 8 * (h2 & 3) + cb; }
    for (unsigned spins = 0; spins < SPIN_CAP; ++spins) {
      const u64 v = __hip_atomic_load(QF + pidx, RLX_AGENT);
      if (__all(v == tag)) break;
      __builtin_amdgcn_s_sleep(8);
    }
    CBAR();
    if (lane == 0) MISC[M_CHK] = 1u;
    unsigned gp[8];
    const int col = 56 * hh + (lane < 56 ? lane : 0);
    for (unsigned spins = 0; spins < SPIN_CAP; ++spins) {
      CBAR();
      bool ok = true;
#pragma unroll
      for (int r = 0; r < 8; ++r) { gp[r] = LD4(W_GP + ((size_t)r * FF + col) * 4); ok &= gp[r] != SENT; }
      if (__all(ok)) break;
      __builtin_amdgcn_s_sleep(16);
    }
    const float gb = rbf((__uint_as_float(gp[0]) + __uint_as_float(gp[1])) + (__uint_as_float(gp[2]) + __uint_as_float(gp[3])));
    const float ub = rbf((__uint_as_float(gp[4]) + __uint_as_float(gp[5])) + (__uint_as_float(gp[6]) + __uint_as_float(gp[7])));
    const float sg = rbf(gb / (1.f + __expf(-gb)));
    const float hv = rbf(sg * ub);
    float acc[8] = {0.f, 0.f, 0.f, 0.f, 0.f, 0.f, 0.f, 0.f};
#pragma unroll
    for (int i = 0; i < 7; ++i) fma8(acc, __shfl(hv, 8 * i + (lane >> 3)), wv[i]);
#pragma unroll
    for (int k = 0; k < 8; ++k) { float t = acc[k]; t += dpp<ROR8>(t); acc[k] = xrow_sum(t); }
    if (lane < 8) {
      const size_t off = W_BP + ((size_t)hh * 512 + cb * 64 + lane * 8) * 4;
      ST16(sane4(f32x4{acc[0], acc[1], acc[2], acc[3]}), off);
      ST16(sane4(f32x4{acc[4], acc[5], acc[6], acc[7]}), off + 16);
    }
  }

  while (MISC[M_CCNT] < 4u) __builtin_amdgcn_s_sleep(2);
  CBAR();
  {
    const int c4 = tid & 31, sg = tid >> 5;
    const size_t pbase = W_PART + (size_t)hh * 32 * PROW;
    v4u o0 = {SENT, SENT, SENT, SENT}, o1 = {SENT, SENT, SENT, SENT}; v2u ml0 = {SENT, SENT}, ml1 = {SENT, SENT};
    STAMP(8);
    for (unsigned spins = 0; spins < SPIN_CAP; ++spins) {
      CBAR();
      if (!valid4(o0) || !valid2(ml0)) { o0 = LD16(pbase + sg * PROW + c4 * 16); ml0 = LD8(pbase + sg * PROW + 512); }
      if (!valid4(o1) || !valid2(ml1)) { o1 = LD16(pbase + (sg + 16) * PROW + c4 * 16); ml1 = LD8(pbase + (sg + 16) * PROW + 512); }
      if (__all(valid4(o0) && valid4(o1) && valid2(ml0) && valid2(ml1))) break;
      __builtin_amdgcn_s_sleep(1);
    }
    STAMP(5);
    const float m0 = __uint_as_float(ml0[0]), m1 = __uint_as_float(ml1[0]);
    const float Mw = x32_max(fmaxf(m0, m1));
    const float e0 = __builtin_amdgcn_exp2f((m0 - Mw) * L2E), e1 = __builtin_amdgcn_exp2f((m1 - Mw) * L2E);
    const float Lw = x32_sum(fmaf(e0, __uint_as_float(ml0[1]), e1 * __uint_as_float(ml1[1])));
    f32x4 o;
#pragma unroll
    for (int k = 0; k < 4; ++k) o[k] = x32_sum(fmaf(e0, __uint_as_float(o0[k]), e1 * __uint_as_float(o1[k])));
    if (lane < 32) *(LAS f32x4*)(L8 + L_RED + wave * 512 + c4 * 16) = o;
    if (lane == 0) *(LAS v2u*)(L8 + L_RML + wave * 8) = v2u{__float_as_uint(Mw), __float_as_uint(Lw)};
    STAMP(9);
    __syncthreads();
    STAMP(12);
  }
  {
    const int j = lane >> 4, p = lane & 15, c2 = lane & 31, hf = lane >> 5;
    const f32x4 mlj = *(const LAS f32x4*)(L8 + L_RML + j * 16);
    const float r0 = *(const LAS float*)(L8 + L_RED + (2 * j) * 512 + (16 * wave + p) * 4);
    const float r1 = *(const LAS float*)(L8 + L_RED + (2 * j + 1) * 512 + (16 * wave + p) * 4);
    unsigned ww[8];
#pragma unroll
    for (int i = 0; i < 8; ++i) ww[i] = *(const LAS unsigned*)(L8 + L_WO + (2 * wave + hf) * BLK + i * 128 + c2 * 4);
    const float M = xrow_max(fmaxf(mlj[0], mlj[2]));
    const float f0 = __builtin_amdgcn_exp2f((mlj[0] - M) * L2E), f1 = __builtin_amdgcn_exp2f((mlj[2] - M) * L2E);
    const float L = xrow_sum(fmaf(f0, mlj[1], f1 * mlj[3]));
    const float ov = rbf(xrow_sum(fmaf(f0, r0, f1 * r1)) / L);
    const float ovr = dpp<ROR8>(ov);
    const float o2 = hf ? ovr : ov;
    float a0 = 0.f, a1 = 0.f;
#define WOSTEP(i) { const float oi = dpp<NEWBCAST + (i)>(o2); a0 = fmaf(oi, bf2f(ww[i] & 0xffffu), a0); a1 = fmaf(oi, bfhi(ww[i]), a1); }
    WOSTEP(0) WOSTEP(1) WOSTEP(2) WOSTEP(3) WOSTEP(4) WOSTEP(5) WOSTEP(6) WOSTEP(7)
#undef WOSTEP
    a0 = x32_sum(a0); a1 = x32_sum(a1);
    if (lane < 32) *(LAS v2u*)(L8 + L_RED2 + wave * 256 + c2 * 8) = v2u{__float_as_uint(a0), __float_as_uint(a1)};
    STAMP(13);
    __syncthreads();
  }
  if (wave == 0) {
    {
      f32x4 a[8];
#pragma unroll
      for (int w = 0; w < 8; ++w) a[w] = *(const LAS f32x4*)(L8 + L_RED2 + w * 256 + (lane & 15) * 16);
      const f32x4 t = ((a[0] + a[1]) + (a[2] + a[3])) + ((a[4] + a[5]) + (a[6] + a[7]));
      if (lane < 16) ST16(sane4(t), W_AP + ((size_t)hh * 512 + cb * 64) * 4 + lane * 16);
    }
    STAMP(6);
    while (MISC[M_CHK] == 0u) __builtin_amdgcn_s_sleep(1);
    CBAR();
    const int col = 64 * cb + 2 * hh;
    const unsigned xw = *(const GAS unsigned*)(x + col);
    const size_t off = (lane < 32 ? W_AP : W_BP) + ((size_t)(lane & 31) * 512 + col) * 4;
    v2u v = {SENT, SENT};
    for (unsigned spins = 0; spins < SPIN_CAP; ++spins) {
      CBAR();
      if (!valid2(v)) v = LD8(off);
      if (__all(valid2(v))) break;
      __builtin_amdgcn_s_sleep(1);
    }
    STAMP(7);
    float r0 = __uint_as_float(v[0]), r1 = __uint_as_float(v[1]);
    r0 += dpp<ROR1>(r0); r0 += dpp<ROR2>(r0); r0 += dpp<ROR4>(r0); r0 += dpp<ROR8>(r0); r0 = x16_sum(r0);
    r1 += dpp<ROR1>(r1); r1 += dpp<ROR2>(r1); r1 += dpp<ROR4>(r1); r1 += dpp<ROR8>(r1); r1 = x16_sum(r1);
    const float B0 = __shfl(r0, 32), B1 = __shfl(r1, 32);
    const float y0 = rbf(bf2f(xw & 0xffffu) + rbf(r0)) + rbf(B0);
    const float y1 = rbf(bfhi(xw) + rbf(r1)) + rbf(B1);
    if (lane == 0) {
      *(GAS unsigned*)(out + col) = cvtpk(y0, y1);
      if (b == 0) {
        const u64 tag = ((u64)MISC[M_TAGHI] << 32) | (u64)MISC[M_TAGLO];
        __hip_atomic_store((gu64*)(ws + W_E), tag, RLX_AGENT);
      }
    }
  }
}

extern "C" void kernel_launch(void* const* d_in, const int* in_sizes, int n_in,
                              void* d_out, int out_size, void* d_ws, size_t ws_size,
                              hipStream_t stream) {
  fused_kernel<<<256, 512, 0, stream>>>((const u16*)d_in[0], (const u16*)d_in[1], (const u16*)d_in[2], (const u16*)d_in[3], (const u16*)d_in[4],
                                        (const u16*)d_in[6], (const u16*)d_in[7], (const u16*)d_in[8], (u16*)d_out, (unsigned char*)d_ws);
}
```

```cpp
#include <hip/hip_runtime.h>
#include <hip/hip_bf16.h>
#include <cstdint>

#define GAS __attribute__((address_space(1)))
#define LAS __attribute__((address_space(3)))
typedef unsigned short u16;
typedef unsigned long long u64;
typedef unsigned v4u __attribute__((ext_vector_type(4)));
typedef unsigned v2u __attribute__((ext_vector_type(2)));
typedef float f32x4 __attribute__((ext_vector_type(4)));
typedef __bf16 bf16x8 __attribute__((ext_vector_type(8)));
typedef short s16x4 __attribute__((ext_vector_type(4)));
typedef GAS unsigned gu32;
typedef GAS u64 gu64;
#define RLX_AGENT __ATOMIC_RELAXED, __HIP_MEMORY_SCOPE_AGENT

constexpr int SK = 65536, FF = 1792;
constexpr int BLK = 1056, SLOT = 16 * BLK, NSLOT = 9;
constexpr int NTILE = 32, NFILL = 2 * NTILE + 1;
constexpr int L_CTRL = NSLOT * SLOT;
constexpr int L_FULL = L_CTRL;
constexpr int L_MISC = L_CTRL + 64;
constexpr int L_FREE = L_CTRL + 160;
constexpr int L_ML = L_CTRL + 320;
constexpr int L_QP = L_CTRL + 512;
constexpr int L_QL = L_QP + 1024;
constexpr int L_XN = L_QL + 1024;
constexpr int L_MRG = L_XN + 1024;
constexpr int L_TOTAL = L_MRG + 8192;
static_assert(L_TOTAL <= 163840, "lds");
constexpr int M_CLR = 0, M_QCNT = 1, M_QRDY = 2, M_TAGLO = 4, M_TAGHI = 5, M_CHK = 6, M_CCNT = 10;
constexpr int L_WO = ((NFILL - 1) % NSLOT) * SLOT;
constexpr int L_RED = (((NFILL - 1) + 4) % NSLOT) * SLOT;
constexpr int L_RML = L_RED + 4096, L_RED2 = L_RED + 4096 + 64;
constexpr size_t W_E = 0;
constexpr size_t W_QF = 4096;
constexpr size_t W_QPG = 8192;
constexpr size_t W_GP = W_QPG + 65536;
constexpr size_t W_BP = W_GP + 65536;
constexpr size_t W_AP = W_BP + 65536;
constexpr int PROW = 640;
constexpr size_t W_PART = W_AP + 65536;
constexpr size_t W_END = W_PART + (size_t)32 * 32 * PROW;
constexpr size_t W_STAMP = 4u << 20;
#define STAMP(k) do { } while (0)
constexpr unsigned SENT = 0xFFFFFFFFu;
constexpr unsigned SPIN_CAP = 1u << 20;

__device__ __forceinline__ float bf2f(unsigned b) { return __builtin_bit_cast(float, b << 16); }
__device__ __forceinline__ float bfhi(unsigned w) { return __builtin_bit_cast(float, w & 0xffff0000u); }
__device__ __forceinline__ unsigned cvtpk(float lo, float hi) {
  typedef __bf16 bf16x2 __attribute__((ext_vector_type(2)));
  typedef float f32x2 __attribute__((ext_vector_type(2)));
  f32x2 v = {lo, hi};
  return __builtin_bit_cast(unsigned, __builtin_convertvector(v, bf16x2));
}
__device__ __forceinline__ float rbf(float f) { return bf2f(cvtpk(f, 0.f) & 0xffffu); }
__device__ __forceinline__ unsigned sane(float f) { const unsigned u = __builtin_bit_cast(unsigned, f); return u == SENT ? 0x7FC00000u : u; }
__device__ __forceinline__ v4u sane4(f32x4 a) { return v4u{sane(a[0]), sane(a[1]), sane(a[2]), sane(a[3])}; }
__device__ __forceinline__ bool valid4(v4u a) { return a[0] != SENT && a[1] != SENT && a[2] != SENT && a[3] != SENT; }
__device__ __forceinline__ bool valid2(v2u a) { return a[0] != SENT && a[1] != SENT; }
template <int CTRL> __device__ __forceinline__ float dpp(float x) {
  return __builtin_bit_cast(float, __builtin_amdgcn_mov_dpp(__builtin_bit_cast(int, x), CTRL, 0xf, 0xf, true));
}
constexpr int ROR1 = 0x121, ROR2 = 0x122, ROR4 = 0x124, ROR8 = 0x128, NEWBCAST = 0x150;
__device__ __forceinline__ float x32_sum(float x) {
  auto t = __builtin_amdgcn_permlane32_swap(__float_as_uint(x), __float_as_uint(x), false, false);
  return __uint_as_float(t[0]) + __uint_as_float(t[1]);
}
__device__ __forceinline__ float x16_sum(float x) {
  auto t = __builtin_amdgcn_permlane16_swap(__float_as_uint(x), __float_as_uint(x), false, false);
  return __uint_as_float(t[0]) + __uint_as_float(t[1]);
}
__device__ __forceinline__ float x32_max(float x) {
  auto t = __builtin_amdgcn_permlane32_swap(__float_as_uint(x), __float_as_uint(x), false, false);
  return fmaxf(__uint_as_float(t[0]), __uint_as_float(t[1]));
}
__device__ __forceinline__ float x16_max(float x) {
  auto t = __builtin_amdgcn_permlane16_swap(__float_as_uint(x), __float_as_uint(x), false, false);
  return fmaxf(__uint_as_float(t[0]), __uint_as_float(t[1]));
}
__device__ __forceinline__ float xrow_max(float x) { return x32_max(x16_max(x)); }
__device__ __forceinline__ float xrow_sum(float x) { return x32_sum(x16_sum(x)); }
__device__ __forceinline__ float wave_sum(float x) {
  x += dpp<ROR1>(x); x += dpp<ROR2>(x); x += dpp<ROR4>(x); x += dpp<ROR8>(x);
  return xrow_sum(x);
}
__device__ __forceinline__ v4u ldnt(const void* p) { return __builtin_nontemporal_load((const GAS v4u*)p); }
#define LDSW() asm volatile("s_waitcnt lgkmcnt(0)" ::: "memory")
#define VMW0() asm volatile("s_waitcnt vmcnt(0)" ::: "memory")
#define CBAR() asm volatile("" ::: "memory")
#define GLDS(src, dst) __builtin_amdgcn_global_load_lds((const unsigned*)(src), (unsigned*)(dst), 16, 0, 2)
#define ST16(v, off) __builtin_amdgcn_raw_buffer_store_b128((v), rs, (int)(off), 0, 16)
#define LD16(off) __builtin_amdgcn_raw_buffer_load_b128(rs, (int)(off), 0, 16)
#define LD8(off) __builtin_amdgcn_raw_buffer_load_b64(rs, (int)(off), 0, 16)
#define LD4(off) __builtin_amdgcn_raw_buffer_load_b32(rs, (int)(off), 0, 16)

__device__ __forceinline__ void fma8(float (&acc)[8], float xv, v4u w) {
#pragma unroll
  for (int k = 0; k < 4; ++k) { acc[2 * k] = fmaf(xv, bf2f(w[k] & 0xffffu), acc[2 * k]); acc[2 * k + 1] = fmaf(xv, bfhi(w[k]), acc[2 * k + 1]); }
}

__global__ void __launch_bounds__(512, 2) fused_kernel(const u16* __restrict__ x, const u16* __restrict__ kc, const u16* __restrict__ vc,
                                                       const u16* __restrict__ wq, const u16* __restrict__ wo, const u16* __restrict__ w1,
                                                       const u16* __restrict__ w3, const u16* __restrict__ w2, u16* __restrict__ out,
                                                       unsigned char* ws) {
  __shared__ __attribute__((aligned(16))) unsigned char lds[L_TOTAL];
  LAS unsigned char* L8 = (LAS unsigned char*)lds;
  volatile LAS unsigned* FULL = (volatile LAS unsigned*)(L8 + L_FULL);
  volatile LAS unsigned* MISC = (volatile LAS unsigned*)(L8 + L_MISC);
  const int tid = threadIdx.x, lane = tid & 63, wave = __builtin_amdgcn_readfirstlane(tid >> 6);
#define K_OPT1 1
#define K_OPT2 0
  const int b = blockIdx.x;
  const int g = K_OPT1 == 0 ? (b & 7) : K_OPT1 == 1 ? (b >> 5) : K_OPT1 == 2 ? (b >> 5) : K_OPT1 == 3 ? ((b >> 3) & 7) : K_OPT1 == 4 ? (((b & 7) + (b >> 3)) & 7) : (((b & 7) >> 1) + 4 * ((b >> 3) & 1));
  const int s = K_OPT1 == 0 ? (b >> 3) : K_OPT1 == 1 ? (b & 31) : K_OPT1 == 2 ? ((b & 7) * 4 + ((b >> 3) & 3)) : K_OPT1 == 3 ? ((b & 7) + 8 * (b >> 6)) : K_OPT1 == 4 ? (b >> 3) : ((b & 1) * 16 + (b >> 4));
  const int gp = b >> 5, sp = b & 31;
  const int hh = b >> 3, cb = b & 7;
  gu64* QF = (gu64*)(ws + W_QF);
  const __amdgpu_buffer_rsrc_t rs = __builtin_amdgcn_make_buffer_rsrc(ws, 0, (int)W_END, 0x00020000);
  constexpr float SCALE = 0.08838834764831845f, L2E = 1.4426950408889634f;

  if (wave == 1) { ((LAS unsigned*)(L8 + L_CTRL))[lane] = 0u; if (lane < 16) ((LAS unsigned*)(L8 + L_CTRL))[64 + lane] = 0u; }

  if (wave == 0) {
    const size_t kvoff = ((size_t)g * SK + (size_t)s * (NTILE * 64)) * 256;
    const unsigned char* kp = (const unsigned char*)kc + kvoff + lane * 16;
    const unsigned char* vp = (const unsigned char*)vc + kvoff + lane * 16;
    const unsigned char* wop = (const unsigned char*)wo + ((size_t)(128 * hh + (lane >> 3)) * 512 + cb * 64) * 2 + (lane & 7) * 16;
    int pub = 0, slot = 0, pslot = 0;
    LDSW(); __builtin_amdgcn_s_barrier();
#define FREE_MIN(sl) ({ const v4u fr_ = *(volatile LAS v4u*)(L8 + L_FREE + (sl) * 16); min(min(fr_[0], fr_[1]), min(fr_[2], fr_[3])); })
    for (int f = 0; f < NFILL; ++f) {
      const bool last = f == NFILL - 1;
      if (f >= NSLOT) {
        const unsigned need = (unsigned)(f - NSLOT + 1);
        if (FREE_MIN(slot) < need) {
          VMW0();
          for (; pub < f; ++pub) { FULL[pslot] = (unsigned)(pub + 1); pslot = pslot == NSLOT - 1 ? 0 : pslot + 1; }
          while (FREE_MIN(slot) < need) __builtin_amdgcn_s_sleep(1);
        }
        CBAR();
      }
      const unsigned char* src = last ? wop : (((f & 1) ? vp : kp) + (size_t)(f >> 1) * 16384);
      const int sstr = last ? 8192 : 1024;
      unsigned char* dst = lds + slot * SLOT;
#define K_OPT3 2
#define K_OPT4 2
      if (f & 1) {
#pragma unroll
        for (int j = 0; j < 16; ++j) __builtin_amdgcn_global_load_lds((const unsigned*)(src + (size_t)j * sstr), (unsigned*)(dst + j * BLK), 16, 0, K_OPT4);
      } else {
#pragma unroll
        for (int j = 0; j < 16; ++j) __builtin_amdgcn_global_load_lds((const unsigned*)(src + (size_t)j * sstr), (unsigned*)(dst + j * BLK), 16, 0, K_OPT3);
      }
      slot = slot == NSLOT - 1 ? 0 : slot + 1;
      if (f + 1 - pub > 2) {
        asm volatile("s_waitcnt vmcnt(32)" ::: "memory");
        FULL[pslot] = (unsigned)(pub + 1); ++pub; pslot = pslot == NSLOT - 1 ? 0 : pslot + 1;
      }
    }
    if (pub + 2 == NFILL) {
      asm volatile("s_waitcnt vmcnt(16)" ::: "memory");
      FULL[pslot] = (unsigned)(pub + 1);
    }
    STAMP(3);
  } else if (wave <= 4) {
    const int c = wave - 1;
    {
      const v4u xs = *(const GAS v4u*)(x + 8 * lane);
      v4u wv[4];
      const int r0 = 128 * (sp >> 3) + 32 * c + (lane >> 3);
#pragma unroll
      for (int i = 0; i < 4; ++i) wv[i] = ldnt(wq + (size_t)(r0 + 8 * i) * 4096 + 512 * gp + 64 * (sp & 7) + 8 * (lane & 7));
      LDSW(); __builtin_amdgcn_s_barrier();
      float xv[8];
#pragma unroll
      for (int k = 0; k < 4; ++k) { xv[2 * k] = bf2f(xs[k] & 0xffffu); xv[2 * k + 1] = bfhi(xs[k]); }
      float sm = 0.f;
#pragma unroll
      for (int i = 0; i < 8; ++i) sm += xv[i];
      const float mean = wave_sum(sm) * (1.f / 512.f);
      float vv = 0.f;
#pragma unroll
      for (int i = 0; i < 8; ++i) { const float d = xv[i] - mean; vv = fmaf(d, d, vv); }
      const float var = wave_sum(vv) * (1.f / 512.f);
      const float mb = rbf(mean), den = rbf(__builtin_sqrtf(rbf(rbf(var) + 1e-5f)));
      v4u xn;
#pragma unroll
      for (int k = 0; k < 4; ++k) xn[k] = cvtpk(rbf(xv[2 * k] - mb) / den, rbf(xv[2 * k + 1] - mb) / den);
      *(LAS v4u*)(L8 + L_XN + lane * 16) = xn;
      LDSW();
      float acc[8] = {0.f, 0.f, 0.f, 0.f, 0.f, 0.f, 0.f, 0.f};
#pragma unroll
      for (int i = 0; i < 4; ++i) fma8(acc, bf2f(((const LAS u16*)(L8 + L_XN))[r0 + 8 * i]), wv[i]);
#pragma unroll
      for (int k = 0; k < 8; ++k) { float t = acc[k]; t += dpp<ROR8>(t); acc[k] = xrow_sum(t); }
      if (lane < 8) {
        LAS f32x4* qp = (LAS f32x4*)(L8 + L_QP + c * 256 + lane * 32);
        qp[0] = f32x4{acc[0], acc[1], acc[2], acc[3]}; qp[1] = f32x4{acc[4], acc[5], acc[6], acc[7]};
      }
      LDSW();
      unsigned old = 0;
      if (lane == 0) old = __hip_atomic_fetch_add((LAS unsigned*)(L8 + L_MISC + 4 * M_QCNT), 1u, __ATOMIC_RELAXED, __HIP_MEMORY_SCOPE_WORKGROUP);
      old = __builtin_amdgcn_readfirstlane(old);
      CBAR();
      if (old == 3u) {
        if (lane < 16) {
          const LAS f32x4* qp = (const LAS f32x4*)(L8 + L_QP + lane * 16);
          const f32x4 qv = (qp[0] + qp[16]) + (qp[32] + qp[48]);
          ST16(__builtin_bit_cast(v4u, qv), W_QPG + ((size_t)(gp * 4 + (sp >> 3)) * 512 + 64 * (sp & 7) + 4 * lane) * 4);
        }
        VMW0();
        while (MISC[M_CLR] == 0u) __builtin_amdgcn_s_sleep(1);
        CBAR();
        const u64 tag = ((u64)MISC[M_TAGHI] << 32) | (u64)MISC[M_TAGLO];
        if (lane == 0) __hip_atomic_store(QF + b, tag, RLX_AGENT);
        STAMP(1);
      }
    }
    const int n = lane & 15, gq = lane >> 4;
    const int kbase = n * BLK + c * 256 + gq * 16;
    const int vbase = (lane >> 2) * BLK + c * 256 + (lane & 3) * 8;
    volatile LAS unsigned* FREEC = (volatile LAS unsigned*)(L8 + L_FREE + c * 4);
    bf16x8 qf[4];
    f32x4 O[8];
#pragma unroll
    for (int d = 0; d < 8; ++d) O[d] = f32x4{0.f, 0.f, 0.f, 0.f};
    float m = -INFINITY, l = 0.f;
#define READ_K(kf, fK) do { const int sK_ = (fK) % NSLOT; \
      _Pragma("unroll") for (int ks = 0; ks < 4; ++ks) kf[ks] = *(const LAS bf16x8*)(L8 + sK_ * SLOT + kbase + ks * 64); \
      LDSW(); FREEC[sK_ * 4] = (unsigned)((fK) + 1); \
      asm volatile("" : "+v"(kf[0]), "+v"(kf[1]), "+v"(kf[2]), "+v"(kf[3]) :: "memory"); } while (0)
#define READ_V(vf, fV) do { const int sV_ = (fV) % NSLOT; \
      while (FULL[sV_] < (unsigned)((fV) + 1)) { if (K_OPT2) __builtin_amdgcn_s_sleep(K_OPT2); } \
      CBAR(); \
      _Pragma("unroll") for (int d = 0; d < 8; ++d) vf[d] = __builtin_amdgcn_ds_read_tr16_b64_v4i16((LAS s16x4*)(L8 + sV_ * SLOT + vbase + d * 32)); \
      LDSW(); FREEC[sV_ * 4] = (unsigned)((fV) + 1); \
      asm volatile("" : "+v"(vf[0]), "+v"(vf[1]), "+v"(vf[2]), "+v"(vf[3]), "+v"(vf[4]), "+v"(vf[5]), "+v"(vf[6]), "+v"(vf[7]) :: "memory"); } while (0)
    auto qk_softmax = [&](const bf16x8 (&kf)[4]) __attribute__((always_inline)) -> s16x4 {
      f32x4 S0 = {0.f, 0.f, 0.f, 0.f}, S1 = {0.f, 0.f, 0.f, 0.f};
      S0 = __builtin_amdgcn_mfma_f32_16x16x32_bf16(kf[0], qf[0], S0, 0, 0, 0);
      S1 = __builtin_amdgcn_mfma_f32_16x16x32_bf16(kf[1], qf[1], S1, 0, 0, 0);
      S0 = __builtin_amdgcn_mfma_f32_16x16x32_bf16(kf[2], qf[2], S0, 0, 0, 0);
      S1 = __builtin_amdgcn_mfma_f32_16x16x32_bf16(kf[3], qf[3], S1, 0, 0, 0);
      float sc[4];
#pragma unroll
      for (int r = 0; r < 4; ++r) sc[r] = rbf(rbf(S0[r] + S1[r]) * SCALE);
      const float pm = xrow_max(fmaxf(fmaxf(sc[0], sc[1]), fmaxf(sc[2], sc[3])));
      if (__any(pm > m)) {
        const float mn = fmaxf(m, pm);
        const float alpha = __builtin_amdgcn_exp2f((m - mn) * L2E);
        m = mn; l *= alpha;
        float al[4];
#pragma unroll
        for (int r = 0; r < 4; ++r) al[r] = __builtin_bit_cast(float, __builtin_amdgcn_readlane(__builtin_bit_cast(int, alpha), r));
#pragma unroll
        for (int d = 0; d < 8; ++d)
#pragma unroll
          for (int r = 0; r < 4; ++r) O[d][r] *= al[r];
      }
      float p[4];
#pragma unroll
      for (int r = 0; r < 4; ++r) p[r] = __builtin_amdgcn_exp2f((sc[r] - m) * L2E);
      l += (p[0] + p[1]) + (p[2] + p[3]);
      return __builtin_bit_cast(s16x4, v2u{cvtpk(p[0], p[1]), cvtpk(p[2], p[3])});
    };
    auto pv = [&](const s16x4 pf, const s16x4 (&vf)[8]) __attribute__((always_inline)) {
#pragma unroll
      for (int d = 0; d < 8; ++d) O[d] = __builtin_amdgcn_mfma_f32_16x16x16bf16_1k(pf, vf[d], O[d], 0, 0, 0);
    };
    {
      bf16x8 k0[4], k1[4], k2[4], k3[4]; s16x4 v0[8], v1[8], v2[8], v3[8];
#define WAITK(f) do { while (FULL[(f) % NSLOT] < (unsigned)((f) + 1)) __builtin_amdgcn_s_sleep(1); CBAR(); } while (0)
      WAITK(0); READ_K(k0, 0); READ_V(v0, 1);
      WAITK(2); READ_K(k1, 2); READ_V(v1, 3);
      WAITK(4); READ_K(k2, 4); READ_V(v2, 5);
      WAITK(6); READ_K(k3, 6); READ_V(v3, 7);
      while (MISC[M_QRDY] == 0u) __builtin_amdgcn_s_sleep(1);
      CBAR();
#pragma unroll
      for (int ks = 0; ks < 4; ++ks) qf[ks] = *(const LAS bf16x8*)(L8 + L_QL + (n & 3) * 256 + ks * 64 + gq * 16);
      pv(qk_softmax(k0), v0); pv(qk_softmax(k1), v1); pv(qk_softmax(k2), v2); pv(qk_softmax(k3), v3);
    }
#pragma unroll 1
    for (int t = 4; t < NTILE; ++t) {
      const int fK = 2 * t, fV = 2 * t + 1;
      while (FULL[fK % NSLOT] < (unsigned)(fK + 1)) { if (K_OPT2) __builtin_amdgcn_s_sleep(K_OPT2); }
      CBAR();
      bf16x8 kf[4]; s16x4 vf[8];
      READ_K(kf, fK);
      const s16x4 pf = qk_softmax(kf);
      READ_V(vf, fV);
      pv(pf, vf);
    }
    STAMP(3);
    const float lt = xrow_sum(l);
    {
      LAS float* mrg = (LAS float*)(L8 + L_MRG + c * 2048 + gq * 512 + n * 4);
#pragma unroll
      for (int d = 0; d < 8; ++d) {
        const float lo = (gq & 1) ? O[d][1] : O[d][0], hi = (gq & 1) ? O[d][3] : O[d][2];
        mrg[d * 16] = (gq & 2) ? hi : lo;
      }
      if (lane < 4) *(LAS v2u*)(L8 + L_ML + lane * 32 + c * 8) = v2u{__float_as_uint(m), __float_as_uint(lt)};
    }
    LDSW();
    if (lane == 0) __hip_atomic_fetch_add((LAS unsigned*)(L8 + L_MISC + 4 * M_CCNT), 1u, __ATOMIC_RELAXED, __HIP_MEMORY_SCOPE_WORKGROUP);
    while (MISC[M_CCNT] < 4u) { }
    CBAR();
    {
      const f32x4 ml01 = *(const LAS f32x4*)(L8 + L_ML + c * 32), ml23 = *(const LAS f32x4*)(L8 + L_ML + c * 32 + 16);
      f32x4 ov[4];
#pragma unroll
      for (int w = 0; w < 4; ++w) ov[w] = *(const LAS f32x4*)(L8 + L_MRG + w * 2048 + c * 512 + (lane & 31) * 16);
      const float M = fmaxf(fmaxf(ml01[0], ml01[2]), fmaxf(ml23[0], ml23[2]));
      const float w0 = __builtin_amdgcn_exp2f((ml01[0] - M) * L2E), w1v = __builtin_amdgcn_exp2f((ml01[2] - M) * L2E);
      const float w2v = __builtin_amdgcn_exp2f((ml23[0] - M) * L2E), w3v = __builtin_amdgcn_exp2f((ml23[2] - M) * L2E);
      const f32x4 o = (w0 * ov[0] + w1v * ov[1]) + (w2v * ov[2] + w3v * ov[3]);
      const float L = (w0 * ml01[1] + w1v * ml01[3]) + (w2v * ml23[1] + w3v * ml23[3]);
      const size_t off = W_PART + ((size_t)(4 * g + c) * 32 + s) * PROW;
      const v4u val = lane < 32 ? sane4(o) : v4u{sane(M), sane(L), 0u, 0u};
      if (lane <= 32) ST16(val, off + lane * 16);
    }
    STAMP(4);
  } else if (wave == 5) {
    const u64 E = __hip_atomic_load((gu64*)(ws + W_E), RLX_AGENT);
    const v4u sv = {SENT, SENT, SENT, SENT};
#pragma unroll
    for (int h = 0; h < 4; ++h)
      if (lane < 33) ST16(sv, W_PART + ((size_t)(4 * g + h) * 32 + s) * PROW + lane * 16);
    if (lane < 16) ST16(sv, W_AP + ((size_t)hh * 512 + cb * 64) * 4 + lane * 16);
    else if (lane < 32) ST16(sv, W_BP + ((size_t)hh * 512 + cb * 64) * 4 + (lane - 16) * 16);
    else if (lane < 48 && sp < 28) ST16(sv, W_GP + ((size_t)gp * FF + sp * 64) * 4 + (lane - 32) * 16);
    LDSW(); __builtin_amdgcn_s_barrier();
    VMW0();
    const u64 tag = E + 1;
    if (lane == 0) { MISC[M_TAGLO] = (unsigned)tag; MISC[M_TAGHI] = (unsigned)(tag >> 32); }
    LDSW();
    if (lane == 0) MISC[M_CLR] = 1u;
    for (unsigned spins = 0; spins < SPIN_CAP; ++spins) {
      const u64 v = __hip_atomic_load(QF + 32 * g + (lane & 31), RLX_AGENT);
      if (__all(v == tag)) break;
      __builtin_amdgcn_s_sleep(2);
    }
    CBAR();
    f32x4 qa[4], qb[4];
#pragma unroll
    for (int r = 0; r < 4; ++r) {
      qa[r] = __builtin_bit_cast(f32x4, LD16(W_QPG + ((size_t)(g * 4 + r) * 512 + 8 * lane) * 4));
      qb[r] = __builtin_bit_cast(f32x4, LD16(W_QPG + ((size_t)(g * 4 + r) * 512 + 8 * lane + 4) * 4));
    }
    const f32x4 q0 = (qa[0] + qa[1]) + (qa[2] + qa[3]), q1 = (qb[0] + qb[1]) + (qb[2] + qb[3]);
    *(LAS v4u*)(L8 + L_QL + lane * 16) = v4u{cvtpk(q0[0], q0[1]), cvtpk(q0[2], q0[3]), cvtpk(q1[0], q1[1]), cvtpk(q1[2], q1[3])};
    LDSW();
    if (lane == 0) MISC[M_QRDY] = 1u;
    STAMP(2);
  } else if (wave == 6) {
    LDSW(); __builtin_amdgcn_s_barrier();
    if (sp < 28) {
      while (MISC[M_QRDY] == 0u) __builtin_amdgcn_s_sleep(4);
      CBAR();
      const u16* W = (gp >= 4) ? w3 : w1;
      const int r0 = 128 * (gp & 3) + (lane >> 3);
      v4u a[16];
#pragma unroll
      for (int i = 0; i < 16; ++i) a[i] = ldnt(W + (size_t)(r0 + 8 * i) * FF + 64 * sp + 8 * (lane & 7));
      float acc[8] = {0.f, 0.f, 0.f, 0.f, 0.f, 0.f, 0.f, 0.f};
#pragma unroll
      for (int i = 0; i < 16; ++i) fma8(acc, bf2f(((const LAS u16*)(L8 + L_XN))[r0 + 8 * i]), a[i]);
#pragma unroll
      for (int k = 0; k < 8; ++k) { float t = acc[k]; t += dpp<ROR8>(t); acc[k] = xrow_sum(t); }
      if (lane < 8) {
        const size_t off = W_GP + ((size_t)gp * FF + sp * 64 + lane * 8) * 4;
        ST16(sane4(f32x4{acc[0], acc[1], acc[2], acc[3]}), off);
        ST16(sane4(f32x4{acc[4], acc[5], acc[6], acc[7]}), off + 16);
      }
    }
  } else {
    LDSW(); __builtin_amdgcn_s_barrier();
    while (MISC[M_QRDY] == 0u) __builtin_amdgcn_s_sleep(4);
    CBAR();
    const u64 tag = ((u64)MISC[M_TAGHI] << 32) | (u64)MISC[M_TAGLO];
    v4u wv[7];
#pragma unroll
    for (int i = 0; i < 7; ++i) wv[i] = ldnt(w2 + (size_t)(56 * hh + 8 * i + (lane >> 3)) * 512 + 64 * cb + 8 * (lane & 7));
    for (unsigned spins = 0; spins < SPIN_CAP; ++spins) {
      bool ok = true;
#pragma unroll
      for (int i = 0; i < 4; ++i) ok &= __hip_atomic_load(QF + 64 * i + lane, RLX_AGENT) == tag;
      if (__all(ok)) break;
      __builtin_amdgcn_s_sleep(16);
    }
    CBAR();
    if (lane == 0) MISC[M_CHK] = 1u;
    unsigned gp[8];
    const int col = 56 * hh + (lane < 56 ? lane : 0);
    for (unsigned spins = 0; spins < SPIN_CAP; ++spins) {
      CBAR();
      bool ok = true;
#pragma unroll
      for (int r = 0; r < 8; ++r) { gp[r] = LD4(W_GP + ((size_t)r * FF + col) * 4); ok &= gp[r] != SENT; }
      if (__all(ok)) break;
      __builtin_amdgcn_s_sleep(16);
    }
    const float gb = rbf((__uint_as_float(gp[0]) + __uint_as_float(gp[1])) + (__uint_as_float(gp[2]) + __uint_as_float(gp[3])));
    const float ub = rbf((__uint_as_float(gp[4]) + __uint_as_float(gp[5])) + (__uint_as_float(gp[6]) + __uint_as_float(gp[7])));
    const float sg = rbf(gb / (1.f + __expf(-gb)));
    const float hv = rbf(sg * ub);
    float acc[8] = {0.f, 0.f, 0.f, 0.f, 0.f, 0.f, 0.f, 0.f};
#pragma unroll
    for (int i = 0; i < 7; ++i) fma8(acc, __shfl(hv, 8 * i + (lane >> 3)), wv[i]);
#pragma unroll
    for (int k = 0; k < 8; ++k) { float t = acc[k]; t += dpp<ROR8>(t); acc[k] = xrow_sum(t); }
    if (lane < 8) {
      const size_t off = W_BP + ((size_t)hh * 512 + cb * 64 + lane * 8) * 4;
      ST16(sane4(f32x4{acc[0], acc[1], acc[2], acc[3]}), off);
      ST16(sane4(f32x4{acc[4], acc[5], acc[6], acc[7]}), off + 16);
    }
  }

  while (MISC[M_CCNT] < 4u || MISC[M_CHK] == 0u) __builtin_amdgcn_s_sleep(2);
  CBAR();
  {
    const int c4 = tid & 31, sg = tid >> 5;
    const size_t pbase = W_PART + (size_t)hh * 32 * PROW;
    v4u o0 = {SENT, SENT, SENT, SENT}, o1 = {SENT, SENT, SENT, SENT}; v2u ml0 = {SENT, SENT}, ml1 = {SENT, SENT};
    STAMP(8);
    for (unsigned spins = 0; spins < SPIN_CAP; ++spins) {
      CBAR();
      if (!valid4(o0) || !valid2(ml0)) { o0 = LD16(pbase + sg * PROW + c4 * 16); ml0 = LD8(pbase + sg * PROW + 512); }
      if (!valid4(o1) || !valid2(ml1)) { o1 = LD16(pbase + (sg + 16) * PROW + c4 * 16); ml1 = LD8(pbase + (sg + 16) * PROW + 512); }
      if (__all(valid4(o0) && valid4(o1) && valid2(ml0) && valid2(ml1))) break;
      __builtin_amdgcn_s_sleep(1);
    }
    STAMP(5);
    const float m0 = __uint_as_float(ml0[0]), m1 = __uint_as_float(ml1[0]);
    const float Mw = x32_max(fmaxf(m0, m1));
    const float e0 = __builtin_amdgcn_exp2f((m0 - Mw) * L2E), e1 = __builtin_amdgcn_exp2f((m1 - Mw) * L2E);
    const float Lw = x32_sum(fmaf(e0, __uint_as_float(ml0[1]), e1 * __uint_as_float(ml1[1])));
    f32x4 o;
#pragma unroll
    for (int k = 0; k < 4; ++k) o[k] = x32_sum(fmaf(e0, __uint_as_float(o0[k]), e1 * __uint_as_float(o1[k])));
    if (lane < 32) *(LAS f32x4*)(L8 + L_RED + wave * 512 + c4 * 16) = o;
    if (lane == 0) *(LAS v2u*)(L8 + L_RML + wave * 8) = v2u{__float_as_uint(Mw), __float_as_uint(Lw)};
    STAMP(9);
    __syncthreads();
    STAMP(12);
  }
  {
    const int j = lane >> 4, p = lane & 15, c2 = lane & 31, hf = lane >> 5;
    const f32x4 mlj = *(const LAS f32x4*)(L8 + L_RML + j * 16);
    const float r0 = *(const LAS float*)(L8 + L_RED + (2 * j) * 512 + (16 * wave + p) * 4);
    const float r1 = *(const LAS float*)(L8 + L_RED + (2 * j + 1) * 512 + (16 * wave + p) * 4);
    unsigned ww[8];
#pragma unroll
    for (int i = 0; i < 8; ++i) ww[i] = *(const LAS unsigned*)(L8 + L_WO + (2 * wave + hf) * BLK + i * 128 + c2 * 4);
    const float M = xrow_max(fmaxf(mlj[0], mlj[2]));
    const float f0 = __builtin_amdgcn_exp2f((mlj[0] - M) * L2E), f1 = __builtin_amdgcn_exp2f((mlj[2] - M) * L2E);
    const float L = xrow_sum(fmaf(f0, mlj[1], f1 * mlj[3]));
    const float ov = rbf(xrow_sum(fmaf(f0, r0, f1 * r1)) / L);
    const float ovr = dpp<ROR8>(ov);
    const float o2 = hf ? ovr : ov;
    float a0 = 0.f, a1 = 0.f;
#define WOSTEP(i) { const float oi = dpp<NEWBCAST + (i)>(o2); a0 = fmaf(oi, bf2f(ww[i] & 0xffffu), a0); a1 = fmaf(oi, bfhi(ww[i]), a1); }
    WOSTEP(0) WOSTEP(1) WOSTEP(2) WOSTEP(3) WOSTEP(4) WOSTEP(5) WOSTEP(6) WOSTEP(7)
#undef WOSTEP
    a0 = x32_sum(a0); a1 = x32_sum(a1);
    if (lane < 32) *(LAS v2u*)(L8 + L_RED2 + wave * 256 + c2 * 8) = v2u{__float_as_uint(a0), __float_as_uint(a1)};
    STAMP(13);
    __syncthreads();
  }
  if (wave == 0) {
    {
      f32x4 a[8];
#pragma unroll
      for (int w = 0; w < 8; ++w) a[w] = *(const LAS f32x4*)(L8 + L_RED2 + w * 256 + (lane & 15) * 16);
      const f32x4 t = ((a[0] + a[1]) + (a[2] + a[3])) + ((a[4] + a[5]) + (a[6] + a[7]));
      if (lane < 16) ST16(sane4(t), W_AP + ((size_t)hh * 512 + cb * 64) * 4 + lane * 16);
    }
    STAMP(6);
    const int col = 2 * b;
    const unsigned xw = *(const GAS unsigned*)(x + col);
    const size_t off = (lane < 32 ? W_AP : W_BP) + ((size_t)(lane & 31) * 512 + col) * 4;
    v2u v = {SENT, SENT};
    for (unsigned spins = 0; spins < SPIN_CAP; ++spins) {
      CBAR();
      if (!valid2(v)) v = LD8(off);
      if (__all(valid2(v))) break;
      __builtin_amdgcn_s_sleep(1);
    }
    STAMP(7);
    float r0 = __uint_as_float(v[0]), r1 = __uint_as_float(v[1]);
    r0 += dpp<ROR1>(r0); r0 += dpp<ROR2>(r0); r0 += dpp<ROR4>(r0); r0 += dpp<ROR8>(r0); r0 = x16_sum(r0);
    r1 += dpp<ROR1>(r1); r1 += dpp<ROR2>(r1); r1 += dpp<ROR4>(r1); r1 += dpp<ROR8>(r1); r1 = x16_sum(r1);
    const float B0 = __shfl(r0, 32), B1 = __shfl(r1, 32);
    const float y0 = rbf(bf2f(xw & 0xffffu) + rbf(r0)) + rbf(B0);
    const float y1 = rbf(bfhi(xw) + rbf(r1)) + rbf(B1);
    if (lane == 0) {
      *(GAS unsigned*)(out + col) = cvtpk(y0, y1);
      if (b == 0) {
        const u64 tag = ((u64)MISC[M_TAGHI] << 32) | (u64)MISC[M_TAGLO];
        __hip_atomic_store((gu64*)(ws + W_E), tag, RLX_AGENT);
      }
    }
  }
}

extern "C" void kernel_launch(void* const* d_in, const int* in_sizes, int n_in,
                              void* d_out, int out_size, void* d_ws, size_t ws_size,
                              hipStream_t stream) {
  fused_kernel<<<256, 512, 0, stream>>>((const u16*)d_in[0], (const u16*)d_in[1], (const u16*)d_in[2], (const u16*)d_in[3], (const u16*)d_in[4],
                                        (const u16*)d_in[6], (const u16*)d_in[7], (const u16*)d_in[8], (u16*)d_out, (unsigned char*)d_ws);
}
```

```cpp
#include <hip/hip_runtime.h>
#include <hip/hip_bf16.h>
#include <cstdint>

#define GAS __attribute__((address_space(1)))
#define LAS __attribute__((address_space(3)))
typedef unsigned short u16;
typedef unsigned long long u64;
typedef unsigned v4u __attribute__((ext_vector_type(4)));
typedef unsigned v2u __attribute__((ext_vector_type(2)));
typedef float f32x4 __attribute__((ext_vector_type(4)));
typedef __bf16 bf16x8 __attribute__((ext_vector_type(8)));
typedef short s16x4 __attribute__((ext_vector_type(4)));
typedef GAS u64 gu64;
#define RLX_AGENT __ATOMIC_RELAXED, __HIP_MEMORY_SCOPE_AGENT

constexpr int SK = 65536, FF = 1792;
constexpr int BLK = 1056;
constexpr int SLOT = 16 * BLK, NSLOT = 9;
constexpr int NTILE = 32, NFILL = 2 * NTILE + 1;
constexpr int L_CTRL = NSLOT * SLOT;
constexpr int L_FULL = L_CTRL;
constexpr int L_MISC = L_CTRL + 64;
constexpr int L_FREE = L_CTRL + 160;
constexpr int L_ML = L_CTRL + 320;
constexpr int L_QP = L_CTRL + 512;
constexpr int L_QL = L_QP + 1024;
constexpr int L_XN = L_QL + 1024;
constexpr int L_MRG = L_XN + 1024;
constexpr int L_TOTAL = L_MRG + 8192;
static_assert(L_TOTAL <= 163840, "lds");
constexpr int M_CLR = 0, M_QCNT = 1, M_QRDY = 2, M_TAGLO = 4, M_TAGHI = 5, M_CHK = 6, M_CCNT = 10;
constexpr int L_WO = ((NFILL - 1) % NSLOT) * SLOT;
constexpr int L_RED = (((NFILL - 1) + 4) % NSLOT) * SLOT;
constexpr int L_RML = L_RED + 4096, L_RED2 = L_RED + 4096 + 64;
constexpr size_t W_E = 0;
constexpr size_t W_QF = 4096;
constexpr size_t W_QPG = 8192;
constexpr size_t W_GP = W_QPG + 65536;
constexpr size_t W_BP = W_GP + 65536;
constexpr size_t W_AP = W_BP + 65536;
constexpr int PROW = 640;
constexpr size_t W_PART = W_AP + 65536;
constexpr size_t W_END = W_PART + (size_t)32 * 32 * PROW;
constexpr unsigned SENT = 0xFFFFFFFFu;
constexpr unsigned SPIN_CAP = 1u << 20;

__device__ __forceinline__ float bf2f(unsigned b) { return __builtin_bit_cast(float, b << 16); }
__device__ __forceinline__ float bfhi(unsigned w) { return __builtin_bit_cast(float, w & 0xffff0000u); }
__device__ __forceinline__ unsigned cvtpk(float lo, float hi) {
  typedef __bf16 bf16x2 __attribute__((ext_vector_type(2)));
  typedef float f32x2 __attribute__((ext_vector_type(2)));
  f32x2 v = {lo, hi};
  return __builtin_bit_cast(unsigned, __builtin_convertvector(v, bf16x2));
}
__device__ __forceinline__ float rbf(float f) { return bf2f(cvtpk(f, 0.f) & 0xffffu); }
__device__ __forceinline__ unsigned sane(float f) { const unsigned u = __builtin_bit_cast(unsigned, f); return u == SENT ? 0x7FC00000u : u; }
__device__ __forceinline__ v4u sane4(f32x4 a) { return v4u{sane(a[0]), sane(a[1]), sane(a[2]), sane(a[3])}; }
__device__ __forceinline__ bool valid4(v4u a) { return a[0] != SENT && a[1] != SENT && a[2] != SENT && a[3] != SENT; }
__device__ __forceinline__ bool valid2(v2u a) { return a[0] != SENT && a[1] != SENT; }
template <int CTRL> __device__ __forceinline__ float dpp(float x) {
  return __builtin_bit_cast(float, __builtin_amdgcn_mov_dpp(__builtin_bit_cast(int, x), CTRL, 0xf, 0xf, true));
}
constexpr int ROR1 = 0x121, ROR2 = 0x122, ROR4 = 0x124, ROR8 = 0x128, NEWBCAST = 0x150;
__device__ __forceinline__ float x32_sum(float x) {
  auto t = __builtin_amdgcn_permlane32_swap(__float_as_uint(x), __float_as_uint(x), false, false);
  return __uint_as_float(t[0]) + __uint_as_float(t[1]);
}
__device__ __forceinline__ float x16_sum(float x) {
  auto t = __builtin_amdgcn_permlane16_swap(__float_as_uint(x), __float_as_uint(x), false, false);
  return __uint_as_float(t[0]) + __uint_as_float(t[1]);
}
__device__ __forceinline__ float x32_max(float x) {
  auto t = __builtin_amdgcn_permlane32_swap(__float_as_uint(x), __float_as_uint(x), false, false);
  return fmaxf(__uint_as_float(t[0]), __uint_as_float(t[1]));
}
__device__ __forceinline__ float x16_max(float x) {
  auto t = __builtin_amdgcn_permlane16_swap(__float_as_uint(x), __float_as_uint(x), false, false);
  return fmaxf(__uint_as_float(t[0]), __uint_as_float(t[1]));
}
__device__ __forceinline__ float xrow_max(float x) { return x32_max(x16_max(x)); }
__device__ __forceinline__ float xrow_sum(float x) { return x32_sum(x16_sum(x)); }
__device__ __forceinline__ float wave_sum(float x) {
  x += dpp<ROR1>(x); x += dpp<ROR2>(x); x += dpp<ROR4>(x); x += dpp<ROR8>(x);
  return xrow_sum(x);
}
__device__ __forceinline__ v4u ldnt(const void* p) { return __builtin_nontemporal_load((const GAS v4u*)p); }
#define LDSW() asm volatile("s_waitcnt lgkmcnt(0)" ::: "memory")
#define VMW0() asm volatile("s_waitcnt vmcnt(0)" ::: "memory")
#define CBAR() asm volatile("" ::: "memory")
#define GLDS(src, dst) __builtin_amdgcn_global_load_lds((const unsigned*)(src), (unsigned*)(dst), 16, 0, 2)
#define ST16(v, off) __builtin_amdgcn_raw_buffer_store_b128((v), rs, (int)(off), 0, 16)
#define LD16(off) __builtin_amdgcn_raw_buffer_load_b128(rs, (int)(off), 0, 16)
#define LD8(off) __builtin_amdgcn_raw_buffer_load_b64(rs, (int)(off), 0, 16)
#define LD4(off) __builtin_amdgcn_raw_buffer_load_b32(rs, (int)(off), 0, 16)

__device__ __forceinline__ void fma8(float (&acc)[8], float xv, v4u w) {
#pragma unroll
  for (int k = 0; k < 4; ++k) { acc[2 * k] = fmaf(xv, bf2f(w[k] & 0xffffu), acc[2 * k]); acc[2 * k + 1] = fmaf(xv, bfhi(w[k]), acc[2 * k + 1]); }
}

__global__ void __launch_bounds__(512, 2) fused_kernel(const u16* __restrict__ x, const u16* __restrict__ kc, const u16* __restrict__ vc,
                                                       const u16* __restrict__ wq, const u16* __restrict__ wo, const u16* __restrict__ w1,
                                                       const u16* __restrict__ w3, const u16* __restrict__ w2, u16* __restrict__ out,
                                                       unsigned char* ws) {
  __shared__ __attribute__((aligned(16))) unsigned char lds[L_TOTAL];
  LAS unsigned char* L8 = (LAS unsigned char*)lds;
  volatile LAS unsigned* FULL = (volatile LAS unsigned*)(L8 + L_FULL);
  volatile LAS unsigned* MISC = (volatile LAS unsigned*)(L8 + L_MISC);
  const int tid = threadIdx.x, lane = tid & 63, wave = __builtin_amdgcn_readfirstlane(tid >> 6);
  const int b = blockIdx.x;
  const int g = b >> 5, s = b & 31;
  const int hh = b >> 3, cb = b & 7;
  gu64* QF = (gu64*)(ws + W_QF);
  const __amdgpu_buffer_rsrc_t rs = __builtin_amdgcn_make_buffer_rsrc(ws, 0, (int)W_END, 0x00020000);
  constexpr float SCALE = 0.08838834764831845f, L2E = 1.4426950408889634f;

  if (wave == 1) { ((LAS unsigned*)(L8 + L_CTRL))[lane] = 0u; if (lane < 16) ((LAS unsigned*)(L8 + L_CTRL))[64 + lane] = 0u; }

  if (wave == 0) {
    const size_t kvoff = ((size_t)g * SK + (size_t)s * (NTILE * 64)) * 256;
    const unsigned char* kp = (const unsigned char*)kc + kvoff + lane * 16;
    const unsigned char* vp = (const unsigned char*)vc + kvoff + lane * 16;
    const unsigned char* wop = (const unsigned char*)wo + ((size_t)(128 * hh + (lane >> 3)) * 512 + cb * 64) * 2 + (lane & 7) * 16;
    int pub = 0, slot = 0, pslot = 0;
    LDSW(); __builtin_amdgcn_s_barrier();
#define FREE_MIN(sl) ({ const v4u fr_ = *(volatile LAS v4u*)(L8 + L_FREE + (sl) * 16); min(min(fr_[0], fr_[1]), min(fr_[2], fr_[3])); })
    for (int f = 0; f < NFILL; ++f) {
      const bool last = f == NFILL - 1;
      if (f >= NSLOT) {
        const unsigned need = (unsigned)(f - NSLOT + 1);
        if (FREE_MIN(slot) < need) {
          VMW0();
          for (; pub < f; ++pub) { FULL[pslot] = (unsigned)(pub + 1); pslot = pslot == NSLOT - 1 ? 0 : pslot + 1; }
          while (FREE_MIN(slot) < need) __builtin_amdgcn_s_sleep(1);
        }
        CBAR();
      }
      const unsigned char* src = last ? wop : (((f & 1) ? vp : kp) + (size_t)(f >> 1) * 16384);
      const int sstr = last ? 8192 : 1024;
      unsigned char* dst = lds + slot * SLOT;
#pragma unroll
      for (int j = 0; j < 16; ++j) GLDS(src + (size_t)j * sstr, dst + j * BLK);
      slot = slot == NSLOT - 1 ? 0 : slot + 1;
      if (f + 1 - pub > 2) {
        asm volatile("s_waitcnt vmcnt(32)" ::: "memory");
        FULL[pslot] = (unsigned)(pub + 1); ++pub; pslot = pslot == NSLOT - 1 ? 0 : pslot + 1;
      }
    }
    if (pub + 2 == NFILL) {
      asm volatile("s_waitcnt vmcnt(16)" ::: "memory");
      FULL[pslot] = (unsigned)(pub + 1);
    }
  } else if (wave <= 4) {
    const int c = wave - 1;
    {
      const v4u xs = *(const GAS v4u*)(x + 8 * lane);
      v4u wv[4];
      const int r0 = 128 * (s >> 3) + 32 * c + (lane >> 3);
#pragma unroll
      for (int i = 0; i < 4; ++i) wv[i] = ldnt(wq + (size_t)(r0 + 8 * i) * 4096 + 512 * g + 64 * (s & 7) + 8 * (lane & 7));
      LDSW(); __builtin_amdgcn_s_barrier();
      float xv[8];
#pragma unroll
      for (int k = 0; k < 4; ++k) { xv[2 * k] = bf2f(xs[k] & 0xffffu); xv[2 * k + 1] = bfhi(xs[k]); }
      float sm = 0.f;
#pragma unroll
      for (int i = 0; i < 8; ++i) sm += xv[i];
      const float mean = wave_sum(sm) * (1.f / 512.f);
      float vv = 0.f;
#pragma unroll
      for (int i = 0; i < 8; ++i) { const float d = xv[i] - mean; vv = fmaf(d, d, vv); }
      const float var = wave_sum(vv) * (1.f / 512.f);
      const float mb = rbf(mean), den = rbf(__builtin_sqrtf(rbf(rbf(var) + 1e-5f)));
      v4u xn;
#pragma unroll
      for (int k = 0; k < 4; ++k) xn[k] = cvtpk(rbf(xv[2 * k] - mb) / den, rbf(xv[2 * k + 1] - mb) / den);
      *(LAS v4u*)(L8 + L_XN + lane * 16) = xn;
      LDSW();
      float acc[8] = {0.f, 0.f, 0.f, 0.f, 0.f, 0.f, 0.f, 0.f};
#pragma unroll
      for (int i = 0; i < 4; ++i) fma8(acc, bf2f(((const LAS u16*)(L8 + L_XN))[r0 + 8 * i]), wv[i]);
#pragma unroll
      for (int k = 0; k < 8; ++k) { float t = acc[k]; t += dpp<ROR8>(t); acc[k] = xrow_sum(t); }
      if (lane < 8) {
        LAS f32x4* qp = (LAS f32x4*)(L8 + L_QP + c * 256 + lane * 32);
        qp[0] = f32x4{acc[0], acc[1], acc[2], acc[3]}; qp[1] = f32x4{acc[4], acc[5], acc[6], acc[7]};
      }
      LDSW();
      unsigned old = 0;
      if (lane == 0) old = __hip_atomic_fetch_add((LAS unsigned*)(L8 + L_MISC + 4 * M_QCNT), 1u, __ATOMIC_RELAXED, __HIP_MEMORY_SCOPE_WORKGROUP);
      old = __builtin_amdgcn_readfirstlane(old);
      CBAR();
      if (old == 3u) {
        if (lane < 16) {
          const LAS f32x4* qp = (const LAS f32x4*)(L8 + L_QP + lane * 16);
          const f32x4 qv = (qp[0] + qp[16]) + (qp[32] + qp[48]);
          ST16(__builtin_bit_cast(v4u, qv), W_QPG + ((size_t)(g * 4 + (s >> 3)) * 512 + 64 * (s & 7) + 4 * lane) * 4);
        }
        VMW0();
        while (MISC[M_CLR] == 0u) __builtin_amdgcn_s_sleep(1);
        CBAR();
        const u64 tag = ((u64)MISC[M_TAGHI] << 32) | (u64)MISC[M_TAGLO];
        if (lane == 0) __hip_atomic_store(QF + b, tag, RLX_AGENT);
      }
    }
    const int n = lane & 15, gq = lane >> 4;
    const int kbase = n * BLK + c * 256 + gq * 16;
    const int vbase = (lane >> 2) * BLK + c * 256 + (lane & 3) * 8;
    volatile LAS unsigned* FREEC = (volatile LAS unsigned*)(L8 + L_FREE + c * 4);
    bf16x8 qf[4];
    f32x4 O[8];
#pragma unroll
    for (int d = 0; d < 8; ++d) O[d] = f32x4{0.f, 0.f, 0.f, 0.f};
    float m = -INFINITY, l = 0.f;
#define READ_K(kf, fK) do { const int sK_ = (fK) % NSLOT; \
      while (FULL[sK_] < (unsigned)((fK) + 1)) { } \
      CBAR(); \
      _Pragma("unroll") for (int ks = 0; ks < 4; ++ks) kf[ks] = *(const LAS bf16x8*)(L8 + sK_ * SLOT + kbase + ks * 64); \
      LDSW(); FREEC[sK_ * 4] = (unsigned)((fK) + 1); \
      asm volatile("" : "+v"(kf[0]), "+v"(kf[1]), "+v"(kf[2]), "+v"(kf[3]) :: "memory"); } while (0)
#define READ_V(vf, fV) do { const int sV_ = (fV) % NSLOT; \
      while (FULL[sV_] < (unsigned)((fV) + 1)) { } \
      CBAR(); \
      _Pragma("unroll") for (int d = 0; d < 8; ++d) vf[d] = __builtin_amdgcn_ds_read_tr16_b64_v4i16((LAS s16x4*)(L8 + sV_ * SLOT + vbase + d * 32)); \
      LDSW(); FREEC[sV_ * 4] = (unsigned)((fV) + 1); \
      asm volatile("" : "+v"(vf[0]), "+v"(vf[1]), "+v"(vf[2]), "+v"(vf[3]), "+v"(vf[4]), "+v"(vf[5]), "+v"(vf[6]), "+v"(vf[7]) :: "memory"); } while (0)
    auto qk_softmax = [&](const bf16x8 (&kf)[4]) __attribute__((always_inline)) -> s16x4 {
      f32x4 S0 = {0.f, 0.f, 0.f, 0.f}, S1 = {0.f, 0.f, 0.f, 0.f};
      S0 = __builtin_amdgcn_mfma_f32_16x16x32_bf16(kf[0], qf[0], S0, 0, 0, 0);
      S1 = __builtin_amdgcn_mfma_f32_16x16x32_bf16(kf[1], qf[1], S1, 0, 0, 0);
      S0 = __builtin_amdgcn_mfma_f32_16x16x32_bf16(kf[2], qf[2], S0, 0, 0, 0);
      S1 = __builtin_amdgcn_mfma_f32_16x16x32_bf16(kf[3], qf[3], S1, 0, 0, 0);
      float sc[4];
#pragma unroll
      for (int r = 0; r < 4; ++r) sc[r] = rbf(rbf(S0[r] + S1[r]) * SCALE);
      const float pm = xrow_max(fmaxf(fmaxf(sc[0], sc[1]), fmaxf(sc[2], sc[3])));
      if (__any(pm > m)) {
        const float mn = fmaxf(m, pm);
        const float alpha = __builtin_amdgcn_exp2f((m - mn) * L2E);
        m = mn; l *= alpha;
        float al[4];
#pragma unroll
        for (int r = 0; r < 4; ++r) al[r] = __builtin_bit_cast(float, __builtin_amdgcn_readlane(__builtin_bit_cast(int, alpha), r));
#pragma unroll
        for (int d = 0; d < 8; ++d)
#pragma unroll
          for (int r = 0; r < 4; ++r) O[d][r] *= al[r];
      }
      float p[4];
#pragma unroll
      for (int r = 0; r < 4; ++r) p[r] = __builtin_amdgcn_exp2f((sc[r] - m) * L2E);
      l += (p[0] + p[1]) + (p[2] + p[3]);
      return __builtin_bit_cast(s16x4, v2u{cvtpk(p[0], p[1]), cvtpk(p[2], p[3])});
    };
    auto pv = [&](const s16x4 pf, const s16x4 (&vf)[8]) __attribute__((always_inline)) {
#pragma unroll
      for (int d = 0; d < 8; ++d) O[d] = __builtin_amdgcn_mfma_f32_16x16x16bf16_1k(pf, vf[d], O[d], 0, 0, 0);
    };
    {
      bf16x8 k0[4], k1[4], k2[4], k3[4]; s16x4 v0[8], v1[8], v2[8], v3[8];
      READ_K(k0, 0); READ_V(v0, 1);
      READ_K(k1, 2); READ_V(v1, 3);
      READ_K(k2, 4); READ_V(v2, 5);
      READ_K(k3, 6); READ_V(v3, 7);
      while (MISC[M_QRDY] == 0u) __builtin_amdgcn_s_sleep(1);
      CBAR();
#pragma unroll
      for (int ks = 0; ks < 4; ++ks) qf[ks] = *(const LAS bf16x8*)(L8 + L_QL + (n & 3) * 256 + ks * 64 + gq * 16);
      pv(qk_softmax(k0), v0); pv(qk_softmax(k1), v1); pv(qk_softmax(k2), v2); pv(qk_softmax(k3), v3);
    }
#pragma unroll 1
    for (int t = 4; t < NTILE; ++t) {
      bf16x8 kf[4]; s16x4 vf[8];
      READ_K(kf, 2 * t);
      const s16x4 pf = qk_softmax(kf);
      READ_V(vf, 2 * t + 1);
      pv(pf, vf);
    }
    const float lt = xrow_sum(l);
    {
      LAS float* mrg = (LAS float*)(L8 + L_MRG + c * 2048 + gq * 512 + n * 4);
#pragma unroll
      for (int d = 0; d < 8; ++d) {
        const float lo = (gq & 1) ? O[d][1] : O[d][0], hi = (gq & 1) ? O[d][3] : O[d][2];
        mrg[d * 16] = (gq & 2) ? hi : lo;
      }
      if (lane < 4) *(LAS v2u*)(L8 + L_ML + lane * 32 + c * 8) = v2u{__float_as_uint(m), __float_as_uint(lt)};
    }
    LDSW();
    if (lane == 0) __hip_atomic_fetch_add((LAS unsigned*)(L8 + L_MISC + 4 * M_CCNT), 1u, __ATOMIC_RELAXED, __HIP_MEMORY_SCOPE_WORKGROUP);
    while (MISC[M_CCNT] < 4u) { }
    CBAR();
    {
      const f32x4 ml01 = *(const LAS f32x4*)(L8 + L_ML + c * 32), ml23 = *(const LAS f32x4*)(L8 + L_ML + c * 32 + 16);
      f32x4 ov[4];
#pragma unroll
      for (int w = 0; w < 4; ++w) ov[w] = *(const LAS f32x4*)(L8 + L_MRG + w * 2048 + c * 512 + (lane & 31) * 16);
      const float M = fmaxf(fmaxf(ml01[0], ml01[2]), fmaxf(ml23[0], ml23[2]));
      const float w0 = __builtin_amdgcn_exp2f((ml01[0] - M) * L2E), w1v = __builtin_amdgcn_exp2f((ml01[2] - M) * L2E);
      const float w2v = __builtin_amdgcn_exp2f((ml23[0] - M) * L2E), w3v = __builtin_amdgcn_exp2f((ml23[2] - M) * L2E);
      const f32x4 o = (w0 * ov[0] + w1v * ov[1]) + (w2v * ov[2] + w3v * ov[3]);
      const float L = (w0 * ml01[1] + w1v * ml01[3]) + (w2v * ml23[1] + w3v * ml23[3]);
      const size_t off = W_PART + ((size_t)(4 * g + c) * 32 + s) * PROW;
      const v4u val = lane < 32 ? sane4(o) : v4u{sane(M), sane(L), 0u, 0u};
      if (lane <= 32) ST16(val, off + lane * 16);
    }
  } else if (wave == 5) {
    const u64 E = __hip_atomic_load((gu64*)(ws + W_E), RLX_AGENT);
    const v4u sv = {SENT, SENT, SENT, SENT};
#pragma unroll
    for (int h = 0; h < 4; ++h)
      if (lane < 33) ST16(sv, W_PART + ((size_t)(4 * g + h) * 32 + s) * PROW + lane * 16);
    if (lane < 16) ST16(sv, W_AP + ((size_t)hh * 512 + cb * 64) * 4 + lane * 16);
    else if (lane < 32) ST16(sv, W_BP + ((size_t)hh * 512 + cb * 64) * 4 + (lane - 16) * 16);
    else if (lane < 48 && s < 28) ST16(sv, W_GP + ((size_t)g * FF + s * 64) * 4 + (lane - 32) * 16);
    LDSW(); __builtin_amdgcn_s_barrier();
    VMW0();
    const u64 tag = E + 1;
    if (lane == 0) { MISC[M_TAGLO] = (unsigned)tag; MISC[M_TAGHI] = (unsigned)(tag >> 32); }
    LDSW();
    if (lane == 0) MISC[M_CLR] = 1u;
    for (unsigned spins = 0; spins < SPIN_CAP; ++spins) {
      const u64 v = __hip_atomic_load(QF + 32 * g + (lane & 31), RLX_AGENT);
      if (__all(v == tag)) break;
      __builtin_amdgcn_s_sleep(2);
    }
    CBAR();
    f32x4 qa[4], qb[4];
#pragma unroll
    for (int r = 0; r < 4; ++r) {
      qa[r] = __builtin_bit_cast(f32x4, LD16(W_QPG + ((size_t)(g * 4 + r) * 512 + 8 * lane) * 4));
      qb[r] = __builtin_bit_cast(f32x4, LD16(W_QPG + ((size_t)(g * 4 + r) * 512 + 8 * lane + 4) * 4));
    }
    const f32x4 q0 = (qa[0] + qa[1]) + (qa[2] + qa[3]), q1 = (qb[0] + qb[1]) + (qb[2] + qb[3]);
    *(LAS v4u*)(L8 + L_QL + lane * 16) = v4u{cvtpk(q0[0], q0[1]), cvtpk(q0[2], q0[3]), cvtpk(q1[0], q1[1]), cvtpk(q1[2], q1[3])};
    LDSW();
    if (lane == 0) MISC[M_QRDY] = 1u;
  } else if (wave == 6) {
    LDSW(); __builtin_amdgcn_s_barrier();
    if (s < 28) {
      while (MISC[M_QRDY] == 0u) __builtin_amdgcn_s_sleep(4);
      CBAR();
      const u16* W = (g >= 4) ? w3 : w1;
      const int r0 = 128 * (g & 3) + (lane >> 3);
      v4u a[16];
#pragma unroll
      for (int i = 0; i < 16; ++i) a[i] = ldnt(W + (size_t)(r0 + 8 * i) * FF + 64 * s + 8 * (lane & 7));
      float acc[8] = {0.f, 0.f, 0.f, 0.f, 0.f, 0.f, 0.f, 0.f};
#pragma unroll
      for (int i = 0; i < 16; ++i) fma8(acc, bf2f(((const LAS u16*)(L8 + L_XN))[r0 + 8 * i]), a[i]);
#pragma unroll
      for (int k = 0; k < 8; ++k) { float t = acc[k]; t += dpp<ROR8>(t); acc[k] = xrow_sum(t); }
      if (lane < 8) {
        const size_t off = W_GP + ((size_t)g * FF + s * 64 + lane * 8) * 4;
        ST16(sane4(f32x4{acc[0], acc[1], acc[2], acc[3]}), off);
        ST16(sane4(f32x4{acc[4], acc[5], acc[6], acc[7]}), off + 16);
      }
    }
  } else {
    LDSW(); __builtin_amdgcn_s_barrier();
    while (MISC[M_QRDY] == 0u) __builtin_amdgcn_s_sleep(4);
    CBAR();
    const u64 tag = ((u64)MISC[M_TAGHI] << 32) | (u64)MISC[M_TAGLO];
    v4u wv[7];
#pragma unroll
    for (int i = 0; i < 7; ++i) wv[i] = ldnt(w2 + (size_t)(56 * hh + 8 * i + (lane >> 3)) * 512 + 64 * cb + 8 * (lane & 7));
    for (unsigned spins = 0; spins < SPIN_CAP; ++spins) {
      bool ok = true;
#pragma unroll
      for (int i = 0; i < 4; ++i) ok &= __hip_atomic_load(QF + 64 * i + lane, RLX_AGENT) == tag;
      if (__all(ok)) break;
      __builtin_amdgcn_s_sleep(16);
    }
    CBAR();
    if (lane == 0) MISC[M_CHK] = 1u;
    unsigned gp[8];
    const int col = 56 * hh + (lane < 56 ? lane : 0);
    for (unsigned spins = 0; spins < SPIN_CAP; ++spins) {
      CBAR();
      bool ok = true;
#pragma unroll
      for (int r = 0; r < 8; ++r) { gp[r] = LD4(W_GP + ((size_t)r * FF + col) * 4); ok &= gp[r] != SENT; }
      if (__all(ok)) break;
      __builtin_amdgcn_s_sleep(16);
    }
    const float gb = rbf((__uint_as_float(gp[0]) + __uint_as_float(gp[1])) + (__uint_as_float(gp[2]) + __uint_as_float(gp[3])));
    const float ub = rbf((__uint_as_float(gp[4]) + __uint_as_float(gp[5])) + (__uint_as_float(gp[6]) + __uint_as_float(gp[7])));
    const float sg = rbf(gb / (1.f + __expf(-gb)));
    const float hv = rbf(sg * ub);
    float acc[8] = {0.f, 0.f, 0.f, 0.f, 0.f, 0.f, 0.f, 0.f};
#pragma unroll
    for (int i = 0; i < 7; ++i) fma8(acc, __shfl(hv, 8 * i + (lane >> 3)), wv[i]);
#pragma unroll
    for (int k = 0; k < 8; ++k) { float t = acc[k]; t += dpp<ROR8>(t); acc[k] = xrow_sum(t); }
    if (lane < 8) {
      const size_t off = W_BP + ((size_t)hh * 512 + cb * 64 + lane * 8) * 4;
      ST16(sane4(f32x4{acc[0], acc[1], acc[2], acc[3]}), off);
      ST16(sane4(f32x4{acc[4], acc[5], acc[6], acc[7]}), off + 16);
    }
  }

  while (MISC[M_CCNT] < 4u || MISC[M_CHK] == 0u) __builtin_amdgcn_s_sleep(2);
  CBAR();
  {
    const int c4 = tid & 31, sg = tid >> 5;
    const size_t pbase = W_PART + (size_t)hh * 32 * PROW;
    v4u o0 = {SENT, SENT, SENT, SENT}, o1 = {SENT, SENT, SENT, SENT}; v2u ml0 = {SENT, SENT}, ml1 = {SENT, SENT};
    for (unsigned spins = 0; spins < SPIN_CAP; ++spins) {
      CBAR();
      if (!valid4(o0) || !valid2(ml0)) { o0 = LD16(pbase + sg * PROW + c4 * 16); ml0 = LD8(pbase + sg * PROW + 512); }
      if (!valid4(o1) || !valid2(ml1)) { o1 = LD16(pbase + (sg + 16) * PROW + c4 * 16); ml1 = LD8(pbase + (sg + 16) * PROW + 512); }
      if (__all(valid4(o0) && valid4(o1) && valid2(ml0) && valid2(ml1))) break;
      __builtin_amdgcn_s_sleep(1);
    }
    const float m0 = __uint_as_float(ml0[0]), m1 = __uint_as_float(ml1[0]);
    const float Mw = x32_max(fmaxf(m0, m1));
    const float e0 = __builtin_amdgcn_exp2f((m0 - Mw) * L2E), e1 = __builtin_amdgcn_exp2f((m1 - Mw) * L2E);
    const float Lw = x32_sum(fmaf(e0, __uint_as_float(ml0[1]), e1 * __uint_as_float(ml1[1])));
    f32x4 o;
#pragma unroll
    for (int k = 0; k < 4; ++k) o[k] = x32_sum(fmaf(e0, __uint_as_float(o0[k]), e1 * __uint_as_float(o1[k])));
    if (lane < 32) *(LAS f32x4*)(L8 + L_RED + wave * 512 + c4 * 16) = o;
    if (lane == 0) *(LAS v2u*)(L8 + L_RML + wave * 8) = v2u{__float_as_uint(Mw), __float_as_uint(Lw)};
    VMW0();
    __syncthreads();
  }
  {
    const int j = lane >> 4, p = lane & 15, c2 = lane & 31, hf = lane >> 5;
    const f32x4 mlj = *(const LAS f32x4*)(L8 + L_RML + j * 16);
    const float r0 = *(const LAS float*)(L8 + L_RED + (2 * j) * 512 + (16 * wave + p) * 4);
    const float r1 = *(const LAS float*)(L8 + L_RED + (2 * j + 1) * 512 + (16 * wave + p) * 4);
    unsigned ww[8];
#pragma unroll
    for (int i = 0; i < 8; ++i) ww[i] = *(const LAS unsigned*)(L8 + L_WO + (2 * wave + hf) * BLK + i * 128 + c2 * 4);
    const float M = xrow_max(fmaxf(mlj[0], mlj[2]));
    const float f0 = __builtin_amdgcn_exp2f((mlj[0] - M) * L2E), f1 = __builtin_amdgcn_exp2f((mlj[2] - M) * L2E);
    const float L = xrow_sum(fmaf(f0, mlj[1], f1 * mlj[3]));
    const float ov = rbf(xrow_sum(fmaf(f0, r0, f1 * r1)) / L);
    const float ovr = dpp<ROR8>(ov);
    const float o2 = hf ? ovr : ov;
    float a0 = 0.f, a1 = 0.f;
#define WOSTEP(i) { const float oi = dpp<NEWBCAST + (i)>(o2); a0 = fmaf(oi, bf2f(ww[i] & 0xffffu), a0); a1 = fmaf(oi, bfhi(ww[i]), a1); }
    WOSTEP(0) WOSTEP(1) WOSTEP(2) WOSTEP(3) WOSTEP(4) WOSTEP(5) WOSTEP(6) WOSTEP(7)
#undef WOSTEP
    a0 = x32_sum(a0); a1 = x32_sum(a1);
    if (lane < 32) *(LAS v2u*)(L8 + L_RED2 + wave * 256 + c2 * 8) = v2u{__float_as_uint(a0), __float_as_uint(a1)};
    __syncthreads();
  }
  if (wave == 0) {
    {
      f32x4 a[8];
#pragma unroll
      for (int w = 0; w < 8; ++w) a[w] = *(const LAS f32x4*)(L8 + L_RED2 + w * 256 + (lane & 15) * 16);
      const f32x4 t = ((a[0] + a[1]) + (a[2] + a[3])) + ((a[4] + a[5]) + (a[6] + a[7]));
      if (lane < 16) ST16(sane4(t), W_AP + ((size_t)hh * 512 + cb * 64) * 4 + lane * 16);
    }
    const int col = 2 * b;
    const unsigned xw = *(const GAS unsigned*)(x + col);
    const size_t off = (lane < 32 ? W_AP : W_BP) + ((size_t)(lane & 31) * 512 + col) * 4;
    v2u v = {SENT, SENT};
    for (unsigned spins = 0; spins < SPIN_CAP; ++spins) {
      CBAR();
      if (!valid2(v)) v = LD8(off);
      if (__all(valid2(v))) break;
      __builtin_amdgcn_s_sleep(1);
    }
    float r0 = __uint_as_float(v[0]), r1 = __uint_as_float(v[1]);
    r0 += dpp<ROR1>(r0); r0 += dpp<ROR2>(r0); r0 += dpp<ROR4>(r0); r0 += dpp<ROR8>(r0); r0 = x16_sum(r0);
    r1 += dpp<ROR1>(r1); r1 += dpp<ROR2>(r1); r1 += dpp<ROR4>(r1); r1 += dpp<ROR8>(r1); r1 = x16_sum(r1);
    const float B0 = __shfl(r0, 32), B1 = __shfl(r1, 32);
    const float y0 = rbf(bf2f(xw & 0xffffu) + rbf(r0)) + rbf(B0);
    const float y1 = rbf(bfhi(xw) + rbf(r1)) + rbf(B1);
    if (lane == 0) {
      *(GAS unsigned*)(out + col) = cvtpk(y0, y1);
      if (b == 0) {
        const u64 tag = ((u64)MISC[M_TAGHI] << 32) | (u64)MISC[M_TAGLO];
        __hip_atomic_store((gu64*)(ws + W_E), tag, RLX_AGENT);
      }
    }
  }
}

extern "C" void kernel_launch(void* const* d_in, const int* in_sizes, int n_in,
                              void* d_out, int out_size, void* d_ws, size_t ws_size,
                              hipStream_t stream) {
  fused_kernel<<<256, 512, 0, stream>>>((const u16*)d_in[0], (const u16*)d_in[1], (const u16*)d_in[2], (const u16*)d_in[3], (const u16*)d_in[4],
                                        (const u16*)d_in[6], (const u16*)d_in[7], (const u16*)d_in[8], (u16*)d_out, (unsigned char*)d_ws);
}
```

```cpp
#include <hip/hip_runtime.h>
#include <hip/hip_bf16.h>
#include <cstdint>

#define GAS __attribute__((address_space(1)))
#define LAS __attribute__((address_space(3)))
typedef unsigned short u16;
typedef unsigned long long u64;
typedef unsigned v4u __attribute__((ext_vector_type(4)));
typedef unsigned v2u __attribute__((ext_vector_type(2)));
typedef float f32x4 __attribute__((ext_vector_type(4)));
typedef __bf16 bf16x8 __attribute__((ext_vector_type(8)));
typedef short s16x4 __attribute__((ext_vector_type(4)));
typedef GAS u64 gu64;
#define RLX_AGENT __ATOMIC_RELAXED, __HIP_MEMORY_SCOPE_AGENT

constexpr int SK = 65536, FF = 1792;
constexpr int BLK = 1056;
constexpr int SLOT = 16 * BLK, NSLOT = 9;
constexpr int NTILE = 32, NFILL = 2 * NTILE + 1;
constexpr int L_CTRL = NSLOT * SLOT;
constexpr int L_FULL = L_CTRL;
constexpr int L_MISC = L_CTRL + 64;
constexpr int L_FREE = L_CTRL + 160;
constexpr int L_ML = L_CTRL + 320;
constexpr int L_QP = L_CTRL + 512;
constexpr int L_QL = L_QP + 1024;
constexpr int L_XN = L_QL + 1024;
constexpr int L_MRG = L_XN + 1024;
constexpr int L_TOTAL = L_MRG + 8192;
static_assert(L_TOTAL <= 163840, "lds");
constexpr int M_CLR = 0, M_QCNT = 1, M_QRDY = 2, M_TAGLO = 4, M_TAGHI = 5, M_CHK = 6, M_CCNT = 10;
constexpr int L_WO = ((NFILL - 1) % NSLOT) * SLOT;
constexpr int L_RED = (((NFILL - 1) + 4) % NSLOT) * SLOT;
constexpr int L_RML = L_RED + 4096, L_RED2 = L_RED + 4096 + 64;
constexpr size_t W_E = 0;
constexpr size_t W_QF = 4096;
constexpr size_t W_QPG = 8192;
constexpr size_t W_GP = W_QPG + 65536;
constexpr size_t W_BP = W_GP + 65536;
constexpr size_t W_AP = W_BP + 65536;
constexpr int PROW = 640;
constexpr size_t W_PART = W_AP + 65536;
constexpr size_t W_END = W_PART + (size_t)32 * 32 * PROW;
constexpr unsigned SENT = 0xFFFFFFFFu;
constexpr unsigned SPIN_CAP = 1u << 20;

__device__ __forceinline__ float bf2f(unsigned b) { return __builtin_bit_cast(float, b << 16); }
__device__ __forceinline__ float bfhi(unsigned w) { return __builtin_bit_cast(float, w & 0xffff0000u); }
__device__ __forceinline__ unsigned cvtpk(float lo, float hi) {
  typedef __bf16 bf16x2 __attribute__((ext_vector_type(2)));
  typedef float f32x2 __attribute__((ext_vector_type(2)));
  f32x2 v = {lo, hi};
  return __builtin_bit_cast(unsigned, __builtin_convertvector(v, bf16x2));
}
__device__ __forceinline__ float rbf(float f) { return bf2f(cvtpk(f, 0.f) & 0xffffu); }
__device__ __forceinline__ unsigned sane(float f) { const unsigned u = __builtin_bit_cast(unsigned, f); return u == SENT ? 0x7FC00000u : u; }
__device__ __forceinline__ v4u sane4(f32x4 a) { return v4u{sane(a[0]), sane(a[1]), sane(a[2]), sane(a[3])}; }
__device__ __forceinline__ bool valid4(v4u a) { return a[0] != SENT && a[1] != SENT && a[2] != SENT && a[3] != SENT; }
__device__ __forceinline__ bool valid2(v2u a) { return a[0] != SENT && a[1] != SENT; }
template <int CTRL> __device__ __forceinline__ float dpp(float x) {
  return __builtin_bit_cast(float, __builtin_amdgcn_mov_dpp(__builtin_bit_cast(int, x), CTRL, 0xf, 0xf, true));
}
constexpr int ROR1 = 0x121, ROR2 = 0x122, ROR4 = 0x124, ROR8 = 0x128, NEWBCAST = 0x150;
__device__ __forceinline__ float x32_sum(float x) {
  auto t = __builtin_amdgcn_permlane32_swap(__float_as_uint(x), __float_as_uint(x), false, false);
  return __uint_as_float(t[0]) + __uint_as_float(t[1]);
}
__device__ __forceinline__ float x16_sum(float x) {
  auto t = __builtin_amdgcn_permlane16_swap(__float_as_uint(x), __float_as_uint(x), false, false);
  return __uint_as_float(t[0]) + __uint_as_float(t[1]);
}
__device__ __forceinline__ float x32_max(float x) {
  auto t = __builtin_amdgcn_permlane32_swap(__float_as_uint(x), __float_as_uint(x), false, false);
  return fmaxf(__uint_as_float(t[0]), __uint_as_float(t[1]));
}
__device__ __forceinline__ float x16_max(float x) {
  auto t = __builtin_amdgcn_permlane16_swap(__float_as_uint(x), __float_as_uint(x), false, false);
  return fmaxf(__uint_as_float(t[0]), __uint_as_float(t[1]));
}
__device__ __forceinline__ float xrow_max(float x) { return x32_max(x16_max(x)); }
__device__ __forceinline__ float xrow_sum(float x) { return x32_sum(x16_sum(x)); }
__device__ __forceinline__ float wave_sum(float x) {
  x += dpp<ROR1>(x); x += dpp<ROR2>(x); x += dpp<ROR4>(x); x += dpp<ROR8>(x);
  return xrow_sum(x);
}
__device__ __forceinline__ v4u ldnt(const void* p) { return __builtin_nontemporal_load((const GAS v4u*)p); }
#define LDSW() asm volatile("s_waitcnt lgkmcnt(0)" ::: "memory")
#define VMW0() asm volatile("s_waitcnt vmcnt(0)" ::: "memory")
#define CBAR() asm volatile("" ::: "memory")
#define GLDS(src, dst) __builtin_amdgcn_global_load_lds((const unsigned*)(src), (unsigned*)(dst), 16, 0, 2)
#define ST16(v, off) __builtin_amdgcn_raw_buffer_store_b128((v), rs, (int)(off), 0, 16)
#define LD16(off) __builtin_amdgcn_raw_buffer_load_b128(rs, (int)(off), 0, 16)
#define LD8(off) __builtin_amdgcn_raw_buffer_load_b64(rs, (int)(off), 0, 16)
#define LD4(off) __builtin_amdgcn_raw_buffer_load_b32(rs, (int)(off), 0, 16)

__device__ __forceinline__ void fma8(float (&acc)[8], float xv, v4u w) {
#pragma unroll
  for (int k = 0; k < 4; ++k) { acc[2 * k] = fmaf(xv, bf2f(w[k] & 0xffffu), acc[2 * k]); acc[2 * k + 1] = fmaf(xv, bfhi(w[k]), acc[2 * k + 1]); }
}

__global__ void __launch_bounds__(512, 2) fused_kernel(const u16* __restrict__ x, const u16* __restrict__ kc, const u16* __restrict__ vc,
                                                       const u16* __restrict__ wq, const u16* __restrict__ wo, const u16* __restrict__ w1,
                                                       const u16* __restrict__ w3, const u16* __restrict__ w2, u16* __restrict__ out,
                                                       unsigned char* ws) {
  __shared__ __attribute__((aligned(16))) unsigned char lds[L_TOTAL];
  LAS unsigned char* L8 = (LAS unsigned char*)lds;
  volatile LAS unsigned* FULL = (volatile LAS unsigned*)(L8 + L_FULL);
  volatile LAS unsigned* MISC = (volatile LAS unsigned*)(L8 + L_MISC);
  const int tid = threadIdx.x, lane = tid & 63, wave = __builtin_amdgcn_readfirstlane(tid >> 6);
  const int b = blockIdx.x;
  const int g = b >> 5, s = b & 31;
  const int hh = b >> 3, cb = b & 7;
  gu64* QF = (gu64*)(ws + W_QF);
  const __amdgpu_buffer_rsrc_t rs = __builtin_amdgcn_make_buffer_rsrc(ws, 0, (int)W_END, 0x00020000);
  constexpr float SCALE = 0.08838834764831845f, L2E = 1.4426950408889634f;

  if (wave == 1) { ((LAS unsigned*)(L8 + L_CTRL))[lane] = 0u; if (lane < 16) ((LAS unsigned*)(L8 + L_CTRL))[64 + lane] = 0u; }

  if (wave == 0) {
    const size_t kvoff = ((size_t)g * SK + (size_t)s * (NTILE * 64)) * 256;
    const unsigned char* kp = (const unsigned char*)kc + kvoff + lane * 16;
    const unsigned char* vp = (const unsigned char*)vc + kvoff + lane * 16;
    const unsigned char* wop = (const unsigned char*)wo + ((size_t)(128 * hh + (lane >> 3)) * 512 + cb * 64) * 2 + (lane & 7) * 16;
    int pub = 0, slot = 0, pslot = 0;
    LDSW(); __builtin_amdgcn_s_barrier();
#define FREE_MIN(sl) ({ const v4u fr_ = *(volatile LAS v4u*)(L8 + L_FREE + (sl) * 16); min(min(fr_[0], fr_[1]), min(fr_[2], fr_[3])); })
    for (int f = 0; f < NFILL; ++f) {
      const bool last = f == NFILL - 1;
      if (f >= NSLOT) {
        const unsigned need = (unsigned)(f - NSLOT + 1);
        if (FREE_MIN(slot) < need) {
          VMW0();
          for (; pub < f; ++pub) { FULL[pslot] = (unsigned)(pub + 1); pslot = pslot == NSLOT - 1 ? 0 : pslot + 1; }
          while (FREE_MIN(slot) < need) __builtin_amdgcn_s_sleep(1);
        }
        CBAR();
      }
      const unsigned char* src = last ? wop : (((f & 1) ? vp : kp) + (size_t)(f >> 1) * 16384);
      const int sstr = last ? 8192 : 1024;
      unsigned char* dst = lds + slot * SLOT;
      const bool dflt = (f & 1) ? (false) : (g >= 4);
      if (dflt) {
#pragma unroll
        for (int j = 0; j < 16; ++j) __builtin_amdgcn_global_load_lds((const unsigned*)(src + (size_t)j * sstr), (unsigned*)(dst + j * BLK), 16, 0, 0);
      } else {
#pragma unroll
        for (int j = 0; j < 16; ++j) GLDS(src + (size_t)j * sstr, dst + j * BLK);
      }
      slot = slot == NSLOT - 1 ? 0 : slot + 1;
      if (f + 1 - pub > 2) {
        asm volatile("s_waitcnt vmcnt(32)" ::: "memory");
        FULL[pslot] = (unsigned)(pub + 1); ++pub; pslot = pslot == NSLOT - 1 ? 0 : pslot + 1;
      }
    }
    if (pub + 2 == NFILL) {
      asm volatile("s_waitcnt vmcnt(16)" ::: "memory");
      FULL[pslot] = (unsigned)(pub + 1);
    }
  } else if (wave <= 4) {
    const int c = wave - 1;
    {
      const v4u xs = *(const GAS v4u*)(x + 8 * lane);
      v4u wv[4];
      const int r0 = 128 * (s >> 3) + 32 * c + (lane >> 3);
#pragma unroll
      for (int i = 0; i < 4; ++i) wv[i] = ldnt(wq + (size_t)(r0 + 8 * i) * 4096 + 512 * g + 64 * (s & 7) + 8 * (lane & 7));
      LDSW(); __builtin_amdgcn_s_barrier();
      float xv[8];
#pragma unroll
      for (int k = 0; k < 4; ++k) { xv[2 * k] = bf2f(xs[k] & 0xffffu); xv[2 * k + 1] = bfhi(xs[k]); }
      float sm = 0.f;
#pragma unroll
      for (int i = 0; i < 8; ++i) sm += xv[i];
      const float mean = wave_sum(sm) * (1.f / 512.f);
      float vv = 0.f;
#pragma unroll
      for (int i = 0; i < 8; ++i) { const float d = xv[i] - mean; vv = fmaf(d, d, vv); }
      const float var = wave_sum(vv) * (1.f / 512.f);
      const float mb = rbf(mean), den = rbf(__builtin_sqrtf(rbf(rbf(var) + 1e-5f)));
      v4u xn;
#pragma unroll
      for (int k = 0; k < 4; ++k) xn[k] = cvtpk(rbf(xv[2 * k] - mb) / den, rbf(xv[2 * k + 1] - mb) / den);
      *(LAS v4u*)(L8 + L_XN + lane * 16) = xn;
      LDSW();
      float acc[8] = {0.f, 0.f, 0.f, 0.f, 0.f, 0.f, 0.f, 0.f};
#pragma unroll
      for (int i = 0; i < 4; ++i) fma8(acc, bf2f(((const LAS u16*)(L8 + L_XN))[r0 + 8 * i]), wv[i]);
#pragma unroll
      for (int k = 0; k < 8; ++k) { float t = acc[k]; t += dpp<ROR8>(t); acc[k] = xrow_sum(t); }
      if (lane < 8) {
        LAS f32x4* qp = (LAS f32x4*)(L8 + L_QP + c * 256 + lane * 32);
        qp[0] = f32x4{acc[0], acc[1], acc[2], acc[3]}; qp[1] = f32x4{acc[4], acc[5], acc[6], acc[7]};
      }
      LDSW();
      unsigned old = 0;
      if (lane == 0) old = __hip_atomic_fetch_add((LAS unsigned*)(L8 + L_MISC + 4 * M_QCNT), 1u, __ATOMIC_RELAXED, __HIP_MEMORY_SCOPE_WORKGROUP);
      old = __builtin_amdgcn_readfirstlane(old);
      CBAR();
      if (old == 3u) {
        if (lane < 16) {
          const LAS f32x4* qp = (const LAS f32x4*)(L8 + L_QP + lane * 16);
          const f32x4 qv = (qp[0] + qp[16]) + (qp[32] + qp[48]);
          ST16(__builtin_bit_cast(v4u, qv), W_QPG + ((size_t)(g * 4 + (s >> 3)) * 512 + 64 * (s & 7) + 4 * lane) * 4);
        }
        VMW0();
        while (MISC[M_CLR] == 0u) __builtin_amdgcn_s_sleep(1);
        CBAR();
        const u64 tag = ((u64)MISC[M_TAGHI] << 32) | (u64)MISC[M_TAGLO];
        if (lane == 0) __hip_atomic_store(QF + b, tag, RLX_AGENT);
      }
    }
    const int n = lane & 15, gq = lane >> 4;
    const int kbase = n * BLK + c * 256 + gq * 16;
    const int vbase = (lane >> 2) * BLK + c * 256 + (lane & 3) * 8;
    volatile LAS unsigned* FREEC = (volatile LAS unsigned*)(L8 + L_FREE + c * 4);
    bf16x8 qf[4];
    f32x4 O[8];
#pragma unroll
    for (int d = 0; d < 8; ++d) O[d] = f32x4{0.f, 0.f, 0.f, 0.f};
    float m = -INFINITY, l = 0.f;
#define READ_K(kf, fK) do { const int sK_ = (fK) % NSLOT; \
      while (FULL[sK_] < (unsigned)((fK) + 1)) { } \
      CBAR(); \
      _Pragma("unroll") for (int ks = 0; ks < 4; ++ks) kf[ks] = *(const LAS bf16x8*)(L8 + sK_ * SLOT + kbase + ks * 64); \
      LDSW(); FREEC[sK_ * 4] = (unsigned)((fK) + 1); \
      asm volatile("" : "+v"(kf[0]), "+v"(kf[1]), "+v"(kf[2]), "+v"(kf[3]) :: "memory"); } while (0)
#define READ_V(vf, fV) do { const int sV_ = (fV) % NSLOT; \
      while (FULL[sV_] < (unsigned)((fV) + 1)) { } \
      CBAR(); \
      _Pragma("unroll") for (int d = 0; d < 8; ++d) vf[d] = __builtin_amdgcn_ds_read_tr16_b64_v4i16((LAS s16x4*)(L8 + sV_ * SLOT + vbase + d * 32)); \
      LDSW(); FREEC[sV_ * 4] = (unsigned)((fV) + 1); \
      asm volatile("" : "+v"(vf[0]), "+v"(vf[1]), "+v"(vf[2]), "+v"(vf[3]), "+v"(vf[4]), "+v"(vf[5]), "+v"(vf[6]), "+v"(vf[7]) :: "memory"); } while (0)
    auto qk_softmax = [&](const bf16x8 (&kf)[4]) __attribute__((always_inline)) -> s16x4 {
      f32x4 S0 = {0.f, 0.f, 0.f, 0.f}, S1 = {0.f, 0.f, 0.f, 0.f};
      S0 = __builtin_amdgcn_mfma_f32_16x16x32_bf16(kf[0], qf[0], S0, 0, 0, 0);
      S1 = __builtin_amdgcn_mfma_f32_16x16x32_bf16(kf[1], qf[1], S1, 0, 0, 0);
      S0 = __builtin_amdgcn_mfma_f32_16x16x32_bf16(kf[2], qf[2], S0, 0, 0, 0);
      S1 = __builtin_amdgcn_mfma_f32_16x16x32_bf16(kf[3], qf[3], S1, 0, 0, 0);
      float sc[4];
#pragma unroll
      for (int r = 0; r < 4; ++r) sc[r] = rbf(rbf(S0[r] + S1[r]) * SCALE);
      const float pm = xrow_max(fmaxf(fmaxf(sc[0], sc[1]), fmaxf(sc[2], sc[3])));
      if (__any(pm > m)) {
        const float mn = fmaxf(m, pm);
        const float alpha = __builtin_amdgcn_exp2f((m - mn) * L2E);
        m = mn; l *= alpha;
        float al[4];
#pragma unroll
        for (int r = 0; r < 4; ++r) al[r] = __builtin_bit_cast(float, __builtin_amdgcn_readlane(__builtin_bit_cast(int, alpha), r));
#pragma unroll
        for (int d = 0; d < 8; ++d)
#pragma unroll
          for (int r = 0; r < 4; ++r) O[d][r] *= al[r];
      }
      float p[4];
#pragma unroll
      for (int r = 0; r < 4; ++r) p[r] = __builtin_amdgcn_exp2f((sc[r] - m) * L2E);
      l += (p[0] + p[1]) + (p[2] + p[3]);
      return __builtin_bit_cast(s16x4, v2u{cvtpk(p[0], p[1]), cvtpk(p[2], p[3])});
    };
    auto pv = [&](const s16x4 pf, const s16x4 (&vf)[8]) __attribute__((always_inline)) {
#pragma unroll
      for (int d = 0; d < 8; ++d) O[d] = __builtin_amdgcn_mfma_f32_16x16x16bf16_1k(pf, vf[d], O[d], 0, 0, 0);
    };
    {
      bf16x8 k0[4], k1[4], k2[4], k3[4]; s16x4 v0[8], v1[8], v2[8], v3[8];
      READ_K(k0, 0); READ_V(v0, 1);
      READ_K(k1, 2); READ_V(v1, 3);
      READ_K(k2, 4); READ_V(v2, 5);
      READ_K(k3, 6); READ_V(v3, 7);
      while (MISC[M_QRDY] == 0u) __builtin_amdgcn_s_sleep(1);
      CBAR();
#pragma unroll
      for (int ks = 0; ks < 4; ++ks) qf[ks] = *(const LAS bf16x8*)(L8 + L_QL + (n & 3) * 256 + ks * 64 + gq * 16);
      pv(qk_softmax(k0), v0); pv(qk_softmax(k1), v1); pv(qk_softmax(k2), v2); pv(qk_softmax(k3), v3);
    }
#pragma unroll 1
    for (int t = 4; t < NTILE; ++t) {
      bf16x8 kf[4]; s16x4 vf[8];
      READ_K(kf, 2 * t);
      const s16x4 pf = qk_softmax(kf);
      READ_V(vf, 2 * t + 1);
      pv(pf, vf);
    }
    const float lt = xrow_sum(l);
    {
      LAS float* mrg = (LAS float*)(L8 + L_MRG + c * 2048 + gq * 512 + n * 4);
#pragma unroll
      for (int d = 0; d < 8; ++d) {
        const float lo = (gq & 1) ? O[d][1] : O[d][0], hi = (gq & 1) ? O[d][3] : O[d][2];
        mrg[d * 16] = (gq & 2) ? hi : lo;
      }
      if (lane < 4) *(LAS v2u*)(L8 + L_ML + lane * 32 + c * 8) = v2u{__float_as_uint(m), __float_as_uint(lt)};
    }
    LDSW();
    if (lane == 0) __hip_atomic_fetch_add((LAS unsigned*)(L8 + L_MISC + 4 * M_CCNT), 1u, __ATOMIC_RELAXED, __HIP_MEMORY_SCOPE_WORKGROUP);
    while (MISC[M_CCNT] < 4u) { }
    CBAR();
    {
      const f32x4 ml01 = *(const LAS f32x4*)(L8 + L_ML + c * 32), ml23 = *(const LAS f32x4*)(L8 + L_ML + c * 32 + 16);
      f32x4 ov[4];
#pragma unroll
      for (int w = 0; w < 4; ++w) ov[w] = *(const LAS f32x4*)(L8 + L_MRG + w * 2048 + c * 512 + (lane & 31) * 16);
      const float M = fmaxf(fmaxf(ml01[0], ml01[2]), fmaxf(ml23[0], ml23[2]));
      const float w0 = __builtin_amdgcn_exp2f((ml01[0] - M) * L2E), w1v = __builtin_amdgcn_exp2f((ml01[2] - M) * L2E);
      const float w2v = __builtin_amdgcn_exp2f((ml23[0] - M) * L2E), w3v = __builtin_amdgcn_exp2f((ml23[2] - M) * L2E);
      const f32x4 o = (w0 * ov[0] + w1v * ov[1]) + (w2v * ov[2] + w3v * ov[3]);
      const float L = (w0 * ml01[1] + w1v * ml01[3]) + (w2v * ml23[1] + w3v * ml23[3]);
      const size_t off = W_PART + ((size_t)(4 * g + c) * 32 + s) * PROW;
      const v4u val = lane < 32 ? sane4(o) : v4u{sane(M), sane(L), 0u, 0u};
      if (lane <= 32) ST16(val, off + lane * 16);
    }
  } else if (wave == 5) {
    const u64 E = __hip_atomic_load((gu64*)(ws + W_E), RLX_AGENT);
    const v4u sv = {SENT, SENT, SENT, SENT};
#pragma unroll
    for (int h = 0; h < 4; ++h)
      if (lane < 33) ST16(sv, W_PART + ((size_t)(4 * g + h) * 32 + s) * PROW + lane * 16);
    if (lane < 16) ST16(sv, W_AP + ((size_t)hh * 512 + cb * 64) * 4 + lane * 16);
    else if (lane < 32) ST16(sv, W_BP + ((size_t)hh * 512 + cb * 64) * 4 + (lane - 16) * 16);
    else if (lane < 48 && s < 28) ST16(sv, W_GP + ((size_t)g * FF + s * 64) * 4 + (lane - 32) * 16);
    LDSW(); __builtin_amdgcn_s_barrier();
    VMW0();
    const u64 tag = E + 1;
    if (lane == 0) { MISC[M_TAGLO] = (unsigned)tag; MISC[M_TAGHI] = (unsigned)(tag >> 32); }
    LDSW();
    if (lane == 0) MISC[M_CLR] = 1u;
    for (unsigned spins = 0; spins < SPIN_CAP; ++spins) {
      const u64 v = __hip_atomic_load(QF + 32 * g + (lane & 31), RLX_AGENT);
      if (__all(v == tag)) break;
      __builtin_amdgcn_s_sleep(2);
    }
    CBAR();
    f32x4 qa[4], qb[4];
#pragma unroll
    for (int r = 0; r < 4; ++r) {
      qa[r] = __builtin_bit_cast(f32x4, LD16(W_QPG + ((size_t)(g * 4 + r) * 512 + 8 * lane) * 4));
      qb[r] = __builtin_bit_cast(f32x4, LD16(W_QPG + ((size_t)(g * 4 + r) * 512 + 8 * lane + 4) * 4));
    }
    const f32x4 q0 = (qa[0] + qa[1]) + (qa[2] + qa[3]), q1 = (qb[0] + qb[1]) + (qb[2] + qb[3]);
    *(LAS v4u*)(L8 + L_QL + lane * 16) = v4u{cvtpk(q0[0], q0[1]), cvtpk(q0[2], q0[3]), cvtpk(q1[0], q1[1]), cvtpk(q1[2], q1[3])};
    LDSW();
    if (lane == 0) MISC[M_QRDY] = 1u;
  } else if (wave == 6) {
    LDSW(); __builtin_amdgcn_s_barrier();
    if (s < 28) {
      while (MISC[M_QRDY] == 0u) __builtin_amdgcn_s_sleep(4);
      CBAR();
      const u16* W = (g >= 4) ? w3 : w1;
      const int r0 = 128 * (g & 3) + (lane >> 3);
      v4u a[16];
#pragma unroll
      for (int i = 0; i < 16; ++i) a[i] = ldnt(W + (size_t)(r0 + 8 * i) * FF + 64 * s + 8 * (lane & 7));
      float acc[8] = {0.f, 0.f, 0.f, 0.f, 0.f, 0.f, 0.f, 0.f};
#pragma unroll
      for (int i = 0; i < 16; ++i) fma8(acc, bf2f(((const LAS u16*)(L8 + L_XN))[r0 + 8 * i]), a[i]);
#pragma unroll
      for (int k = 0; k < 8; ++k) { float t = acc[k]; t += dpp<ROR8>(t); acc[k] = xrow_sum(t); }
      if (lane < 8) {
        const size_t off = W_GP + ((size_t)g * FF + s * 64 + lane * 8) * 4;
        ST16(sane4(f32x4{acc[0], acc[1], acc[2], acc[3]}), off);
        ST16(sane4(f32x4{acc[4], acc[5], acc[6], acc[7]}), off + 16);
      }
    }
  } else {
    LDSW(); __builtin_amdgcn_s_barrier();
    while (MISC[M_QRDY] == 0u) __builtin_amdgcn_s_sleep(4);
    CBAR();
    const u64 tag = ((u64)MISC[M_TAGHI] << 32) | (u64)MISC[M_TAGLO];
    v4u wv[7];
#pragma unroll
    for (int i = 0; i < 7; ++i) wv[i] = ldnt(w2 + (size_t)(56 * hh + 8 * i + (lane >> 3)) * 512 + 64 * cb + 8 * (lane & 7));
    for (unsigned spins = 0; spins < SPIN_CAP; ++spins) {
      bool ok = true;
#pragma unroll
      for (int i = 0; i < 4; ++i) ok &= __hip_atomic_load(QF + 64 * i + lane, RLX_AGENT) == tag;
      if (__all(ok)) break;
      __builtin_amdgcn_s_sleep(16);
    }
    CBAR();
    if (lane == 0) MISC[M_CHK] = 1u;
    unsigned gp[8];
    const int col = 56 * hh + (lane < 56 ? lane : 0);
    for (unsigned spins = 0; spins < SPIN_CAP; ++spins) {
      CBAR();
      bool ok = true;
#pragma unroll
      for (int r = 0; r < 8; ++r) { gp[r] = LD4(W_GP + ((size_t)r * FF + col) * 4); ok &= gp[r] != SENT; }
      if (__all(ok)) break;
      __builtin_amdgcn_s_sleep(16);
    }
    const float gb = rbf((__uint_as_float(gp[0]) + __uint_as_float(gp[1])) + (__uint_as_float(gp[2]) + __uint_as_float(gp[3])));
    const float ub = rbf((__uint_as_float(gp[4]) + __uint_as_float(gp[5])) + (__uint_as_float(gp[6]) + __uint_as_float(gp[7])));
    const float sg = rbf(gb / (1.f + __expf(-gb)));
    const float hv = rbf(sg * ub);
    float acc[8] = {0.f, 0.f, 0.f, 0.f, 0.f, 0.f, 0.f, 0.f};
#pragma unroll
    for (int i = 0; i < 7; ++i) fma8(acc, __shfl(hv, 8 * i + (lane >> 3)), wv[i]);
#pragma unroll
    for (int k = 0; k < 8; ++k) { float t = acc[k]; t += dpp<ROR8>(t); acc[k] = xrow_sum(t); }
    if (lane < 8) {
      const size_t off = W_BP + ((size_t)hh * 512 + cb * 64 + lane * 8) * 4;
      ST16(sane4(f32x4{acc[0], acc[1], acc[2], acc[3]}), off);
      ST16(sane4(f32x4{acc[4], acc[5], acc[6], acc[7]}), off + 16);
    }
  }

  while (MISC[M_CCNT] < 4u || MISC[M_CHK] == 0u) __builtin_amdgcn_s_sleep(2);
  CBAR();
  {
    const int c4 = tid & 31, sg = tid >> 5;
    const size_t pbase = W_PART + (size_t)hh * 32 * PROW;
    v4u o0 = {SENT, SENT, SENT, SENT}, o1 = {SENT, SENT, SENT, SENT}; v2u ml0 = {SENT, SENT}, ml1 = {SENT, SENT};
    for (unsigned spins = 0; spins < SPIN_CAP; ++spins) {
      CBAR();
      if (!valid4(o0) || !valid2(ml0)) { o0 = LD16(pbase + sg * PROW + c4 * 16); ml0 = LD8(pbase + sg * PROW + 512); }
      if (!valid4(o1) || !valid2(ml1)) { o1 = LD16(pbase + (sg + 16) * PROW + c4 * 16); ml1 = LD8(pbase + (sg + 16) * PROW + 512); }
      if (__all(valid4(o0) && valid4(o1) && valid2(ml0) && valid2(ml1))) break;
      __builtin_amdgcn_s_sleep(1);
    }
    const float m0 = __uint_as_float(ml0[0]), m1 = __uint_as_float(ml1[0]);
    const float Mw = x32_max(fmaxf(m0, m1));
    const float e0 = __builtin_amdgcn_exp2f((m0 - Mw) * L2E), e1 = __builtin_amdgcn_exp2f((m1 - Mw) * L2E);
    const float Lw = x32_sum(fmaf(e0, __uint_as_float(ml0[1]), e1 * __uint_as_float(ml1[1])));
    f32x4 o;
#pragma unroll
    for (int k = 0; k < 4; ++k) o[k] = x32_sum(fmaf(e0, __uint_as_float(o0[k]), e1 * __uint_as_float(o1[k])));
    if (lane < 32) *(LAS f32x4*)(L8 + L_RED + wave * 512 + c4 * 16) = o;
    if (lane == 0) *(LAS v2u*)(L8 + L_RML + wave * 8) = v2u{__float_as_uint(Mw), __float_as_uint(Lw)};
    VMW0();
    __syncthreads();
  }
  {
    const int j = lane >> 4, p = lane & 15, c2 = lane & 31, hf = lane >> 5;
    const f32x4 mlj = *(const LAS f32x4*)(L8 + L_RML + j * 16);
    const float r0 = *(const LAS float*)(L8 + L_RED + (2 * j) * 512 + (16 * wave + p) * 4);
    const float r1 = *(const LAS float*)(L8 + L_RED + (2 * j + 1) * 512 + (16 * wave + p) * 4);
    unsigned ww[8];
#pragma unroll
    for (int i = 0; i < 8; ++i) ww[i] = *(const LAS unsigned*)(L8 + L_WO + (2 * wave + hf) * BLK + i * 128 + c2 * 4);
    const float M = xrow_max(fmaxf(mlj[0], mlj[2]));
    const float f0 = __builtin_amdgcn_exp2f((mlj[0] - M) * L2E), f1 = __builtin_amdgcn_exp2f((mlj[2] - M) * L2E);
    const float L = xrow_sum(fmaf(f0, mlj[1], f1 * mlj[3]));
    const float ov = rbf(xrow_sum(fmaf(f0, r0, f1 * r1)) / L);
    const float ovr = dpp<ROR8>(ov);
    const float o2 = hf ? ovr : ov;
    float a0 = 0.f, a1 = 0.f;
#define WOSTEP(i) { const float oi = dpp<NEWBCAST + (i)>(o2); a0 = fmaf(oi, bf2f(ww[i] & 0xffffu), a0); a1 = fmaf(oi, bfhi(ww[i]), a1); }
    WOSTEP(0) WOSTEP(1) WOSTEP(2) WOSTEP(3) WOSTEP(4) WOSTEP(5) WOSTEP(6) WOSTEP(7)
#undef WOSTEP
    a0 = x32_sum(a0); a1 = x32_sum(a1);
    if (lane < 32) *(LAS v2u*)(L8 + L_RED2 + wave * 256 + c2 * 8) = v2u{__float_as_uint(a0), __float_as_uint(a1)};
    __syncthreads();
  }
  if (wave == 0) {
    {
      f32x4 a[8];
#pragma unroll
      for (int w = 0; w < 8; ++w) a[w] = *(const LAS f32x4*)(L8 + L_RED2 + w * 256 + (lane & 15) * 16);
      const f32x4 t = ((a[0] + a[1]) + (a[2] + a[3])) + ((a[4] + a[5]) + (a[6] + a[7]));
      if (lane < 16) ST16(sane4(t), W_AP + ((size_t)hh * 512 + cb * 64) * 4 + lane * 16);
    }
    const int col = 2 * b;
    const unsigned xw = *(const GAS unsigned*)(x + col);
    const size_t off = (lane < 32 ? W_AP : W_BP) + ((size_t)(lane & 31) * 512 + col) * 4;
    v2u v = {SENT, SENT};
    for (unsigned spins = 0; spins < SPIN_CAP; ++spins) {
      CBAR();
      if (!valid2(v)) v = LD8(off);
      if (__all(valid2(v))) break;
      __builtin_amdgcn_s_sleep(1);
    }
    float r0 = __uint_as_float(v[0]), r1 = __uint_as_float(v[1]);
    r0 += dpp<ROR1>(r0); r0 += dpp<ROR2>(r0); r0 += dpp<ROR4>(r0); r0 += dpp<ROR8>(r0); r0 = x16_sum(r0);
    r1 += dpp<ROR1>(r1); r1 += dpp<ROR2>(r1); r1 += dpp<ROR4>(r1); r1 += dpp<ROR8>(r1); r1 = x16_sum(r1);
    const float B0 = __shfl(r0, 32), B1 = __shfl(r1, 32);
    const float y0 = rbf(bf2f(xw & 0xffffu) + rbf(r0)) + rbf(B0);
    const float y1 = rbf(bfhi(xw) + rbf(r1)) + rbf(B1);
    if (lane == 0) {
      *(GAS unsigned*)(out + col) = cvtpk(y0, y1);
      if (b == 0) {
        const u64 tag = ((u64)MISC[M_TAGHI] << 32) | (u64)MISC[M_TAGLO];
        __hip_atomic_store((gu64*)(ws + W_E), tag, RLX_AGENT);
      }
    }
  }
}

extern "C" void kernel_launch(void* const* d_in, const int* in_sizes, int n_in,
                              void* d_out, int out_size, void* d_ws, size_t ws_size,
                              hipStream_t stream) {
  fused_kernel<<<256, 512, 0, stream>>>((const u16*)d_in[0], (const u16*)d_in[1], (const u16*)d_in[2], (const u16*)d_in[3], (const u16*)d_in[4],
                                        (const u16*)d_in[6], (const u16*)d_in[7], (const u16*)d_in[8], (u16*)d_out, (unsigned char*)d_ws);
}
```

```cpp
#include <hip/hip_runtime.h>
#include <hip/hip_bf16.h>
#include <cstdint>

#define GAS __attribute__((address_space(1)))
#define LAS __attribute__((address_space(3)))
typedef unsigned short u16;
typedef unsigned long long u64;
typedef unsigned v4u __attribute__((ext_vector_type(4)));
typedef unsigned v2u __attribute__((ext_vector_type(2)));
typedef float f32x4 __attribute__((ext_vector_type(4)));
typedef __bf16 bf16x8 __attribute__((ext_vector_type(8)));
typedef short s16x4 __attribute__((ext_vector_type(4)));
typedef GAS u64 gu64;
#define RLX_AGENT __ATOMIC_RELAXED, __HIP_MEMORY_SCOPE_AGENT

constexpr int SK = 65536, FF = 1792;
constexpr int BLK = 1056;
constexpr int SLOT = 16 * BLK, NSLOT = 9;
constexpr int NTILE = 32, NFILL = 2 * NTILE + 1;
constexpr int L_CTRL = NSLOT * SLOT;
constexpr int L_FULL = L_CTRL;
constexpr int L_MISC = L_CTRL + 64;
constexpr int L_FREE = L_CTRL + 160;
constexpr int L_ML = L_CTRL + 320;
constexpr int L_QP = L_CTRL + 512;
constexpr int L_QL = L_QP + 1024;
constexpr int L_XN = L_QL + 1024;
constexpr int L_MRG = L_XN + 1024;
constexpr int L_TOTAL = L_MRG + 8192;
static_assert(L_TOTAL <= 163840, "lds");
constexpr int M_CLR = 0, M_QCNT = 1, M_QRDY = 2, M_TAGLO = 4, M_TAGHI = 5, M_CHK = 6, M_CCNT = 10;
constexpr int L_WO = ((NFILL - 1) % NSLOT) * SLOT;
constexpr int L_RED = (((NFILL - 1) + 4) % NSLOT) * SLOT;
constexpr int L_RML = L_RED + 4096, L_RED2 = L_RED + 4096 + 64;
constexpr size_t W_E = 0;
constexpr size_t W_QF = 4096;
constexpr size_t W_QPG = 8192;
constexpr size_t W_GP = W_QPG + 65536;
constexpr size_t W_BP = W_GP + 65536;
constexpr size_t W_AP = W_BP + 65536;
constexpr int PROW = 640;
constexpr size_t W_PART = W_AP + 65536;
constexpr size_t W_END = W_PART + (size_t)32 * 32 * PROW;
constexpr unsigned SENT = 0xFFFFFFFFu;
constexpr unsigned SPIN_CAP = 1u << 20;

__device__ __forceinline__ float bf2f(unsigned b) { return __builtin_bit_cast(float, b << 16); }
__device__ __forceinline__ float bfhi(unsigned w) { return __builtin_bit_cast(float, w & 0xffff0000u); }
__device__ __forceinline__ unsigned cvtpk(float lo, float hi) {
  typedef __bf16 bf16x2 __attribute__((ext_vector_type(2)));
  typedef float f32x2 __attribute__((ext_vector_type(2)));
  f32x2 v = {lo, hi};
  return __builtin_bit_cast(unsigned, __builtin_convertvector(v, bf16x2));
}
__device__ __forceinline__ float rbf(float f) { return bf2f(cvtpk(f, 0.f) & 0xffffu); }
__device__ __forceinline__ unsigned sane(float f) { const unsigned u = __builtin_bit_cast(unsigned, f); return u == SENT ? 0x7FC00000u : u; }
__device__ __forceinline__ v4u sane4(f32x4 a) { return v4u{sane(a[0]), sane(a[1]), sane(a[2]), sane(a[3])}; }
__device__ __forceinline__ bool valid4(v4u a) { return a[0] != SENT && a[1] != SENT && a[2] != SENT && a[3] != SENT; }
__device__ __forceinline__ bool valid2(v2u a) { return a[0] != SENT && a[1] != SENT; }
template <int CTRL> __device__ __forceinline__ float dpp(float x) {
  return __builtin_bit_cast(float, __builtin_amdgcn_mov_dpp(__builtin_bit_cast(int, x), CTRL, 0xf, 0xf, true));
}
constexpr int ROR1 = 0x121, ROR2 = 0x122, ROR4 = 0x124, ROR8 = 0x128, NEWBCAST = 0x150;
__device__ __forceinline__ float x32_sum(float x) {
  auto t = __builtin_amdgcn_permlane32_swap(__float_as_uint(x), __float_as_uint(x), false, false);
  return __uint_as_float(t[0]) + __uint_as_float(t[1]);
}
__device__ __forceinline__ float x16_sum(float x) {
  auto t = __builtin_amdgcn_permlane16_swap(__float_as_uint(x), __float_as_uint(x), false, false);
  return __uint_as_float(t[0]) + __uint_as_float(t[1]);
}
__device__ __forceinline__ float x32_max(float x) {
  auto t = __builtin_amdgcn_permlane32_swap(__float_as_uint(x), __float_as_uint(x), false, false);
  return fmaxf(__uint_as_float(t[0]), __uint_as_float(t[1]));
}
__device__ __forceinline__ float x16_max(float x) {
  auto t = __builtin_amdgcn_permlane16_swap(__float_as_uint(x), __float_as_uint(x), false, false);
  return fmaxf(__uint_as_float(t[0]), __uint_as_float(t[1]));
}
__device__ __forceinline__ float xrow_max(float x) { return x32_max(x16_max(x)); }
__device__ __forceinline__ float xrow_sum(float x) { return x32_sum(x16_sum(x)); }
__device__ __forceinline__ float wave_sum(float x) {
  x += dpp<ROR1>(x); x += dpp<ROR2>(x); x += dpp<ROR4>(x); x += dpp<ROR8>(x);
  return xrow_sum(x);
}
__device__ __forceinline__ v4u ldnt(const void* p) { return __builtin_nontemporal_load((const GAS v4u*)p); }
#define LDSW() asm volatile("s_waitcnt lgkmcnt(0)" ::: "memory")
#define VMW0() asm volatile("s_waitcnt vmcnt(0)" ::: "memory")
#define CBAR() asm volatile("" ::: "memory")
#define GLDS(src, dst) __builtin_amdgcn_global_load_lds((const unsigned*)(src), (unsigned*)(dst), 16, 0, 2)
#define ST16(v, off) __builtin_amdgcn_raw_buffer_store_b128((v), rs, (int)(off), 0, 16)
#define LD16(off) __builtin_amdgcn_raw_buffer_load_b128(rs, (int)(off), 0, 16)
#define LD8(off) __builtin_amdgcn_raw_buffer_load_b64(rs, (int)(off), 0, 16)
#define LD4(off) __builtin_amdgcn_raw_buffer_load_b32(rs, (int)(off), 0, 16)

__device__ __forceinline__ void fma8(float (&acc)[8], float xv, v4u w) {
#pragma unroll
  for (int k = 0; k < 4; ++k) { acc[2 * k] = fmaf(xv, bf2f(w[k] & 0xffffu), acc[2 * k]); acc[2 * k + 1] = fmaf(xv, bfhi(w[k]), acc[2 * k + 1]); }
}

__global__ void __launch_bounds__(512, 2) fused_kernel(const u16* __restrict__ x, const u16* __restrict__ kc, const u16* __restrict__ vc,
                                                       const u16* __restrict__ wq, const u16* __restrict__ wo, const u16* __restrict__ w1,
                                                       const u16* __restrict__ w3, const u16* __restrict__ w2, u16* __restrict__ out,
                                                       unsigned char* ws) {
  __shared__ __attribute__((aligned(16))) unsigned char lds[L_TOTAL];
  LAS unsigned char* L8 = (LAS unsigned char*)lds;
  volatile LAS unsigned* FULL = (volatile LAS unsigned*)(L8 + L_FULL);
  volatile LAS unsigned* MISC = (volatile LAS unsigned*)(L8 + L_MISC);
  const int tid = threadIdx.x, lane = tid & 63, wave = __builtin_amdgcn_readfirstlane(tid >> 6);
  const int b = blockIdx.x;
  const int g = b >> 5, s = b & 31;
  const int hh = b >> 3, cb = b & 7;
  gu64* QF = (gu64*)(ws + W_QF);
  const __amdgpu_buffer_rsrc_t rs = __builtin_amdgcn_make_buffer_rsrc(ws, 0, (int)W_END, 0x00020000);
  constexpr float SCALE = 0.08838834764831845f, L2E = 1.4426950408889634f;

  if (wave == 1) { ((LAS unsigned*)(L8 + L_CTRL))[lane] = 0u; if (lane < 16) ((LAS unsigned*)(L8 + L_CTRL))[64 + lane] = 0u; }

  if (wave == 0) {
    const size_t kvoff = ((size_t)g * SK + (size_t)s * (NTILE * 64)) * 256;
    const unsigned char* kp = (const unsigned char*)kc + kvoff + lane * 16;
    const unsigned char* vp = (const unsigned char*)vc + kvoff + lane * 16;
    const unsigned char* wop = (const unsigned char*)wo + ((size_t)(128 * hh + (lane >> 3)) * 512 + cb * 64) * 2 + (lane & 7) * 16;
    int pub = 0, slot = 0, pslot = 0;
    LDSW(); __builtin_amdgcn_s_barrier();
#define FREE_MIN(sl) ({ const v4u fr_ = *(volatile LAS v4u*)(L8 + L_FREE + (sl) * 16); min(min(fr_[0], fr_[1]), min(fr_[2], fr_[3])); })
    for (int f = 0; f < NFILL; ++f) {
      const bool last = f == NFILL - 1;
      if (f >= NSLOT) {
        const unsigned need = (unsigned)(f - NSLOT + 1);
        if (FREE_MIN(slot) < need) {
          VMW0();
          for (; pub < f; ++pub) { FULL[pslot] = (unsigned)(pub + 1); pslot = pslot == NSLOT - 1 ? 0 : pslot + 1; }
          while (FREE_MIN(slot) < need) __builtin_amdgcn_s_sleep(1);
        }
        CBAR();
      }
      const unsigned char* src = last ? wop : (((f & 1) ? vp : kp) + (size_t)(f >> 1) * 16384);
      const int sstr = last ? 8192 : 1024;
      unsigned char* dst = lds + slot * SLOT;
#pragma unroll
      for (int j = 0; j < 16; ++j) GLDS(src + (size_t)j * sstr, dst + j * BLK);
      slot = slot == NSLOT - 1 ? 0 : slot + 1;
      if (f + 1 - pub > 2) {
        asm volatile("s_waitcnt vmcnt(32)" ::: "memory");
        FULL[pslot] = (unsigned)(pub + 1); ++pub; pslot = pslot == NSLOT - 1 ? 0 : pslot + 1;
      }
    }
    if (pub + 2 == NFILL) {
      asm volatile("s_waitcnt vmcnt(16)" ::: "memory");
      FULL[pslot] = (unsigned)(pub + 1);
    }
  } else if (wave <= 4) {
    const int c = wave - 1;
    {
      const v4u xs = *(const GAS v4u*)(x + 8 * lane);
      v4u wv[4];
      const int r0 = 128 * (s >> 3) + 32 * c + (lane >> 3);
#pragma unroll
      for (int i = 0; i < 4; ++i) wv[i] = ldnt(wq + (size_t)(r0 + 8 * i) * 4096 + 512 * g + 64 * (s & 7) + 8 * (lane & 7));
      LDSW(); __builtin_amdgcn_s_barrier();
      float xv[8];
#pragma unroll
      for (int k = 0; k < 4; ++k) { xv[2 * k] = bf2f(xs[k] & 0xffffu); xv[2 * k + 1] = bfhi(xs[k]); }
      float sm = 0.f;
#pragma unroll
      for (int i = 0; i < 8; ++i) sm += xv[i];
      const float mean = wave_sum(sm) * (1.f / 512.f);
      float vv = 0.f;
#pragma unroll
      for (int i = 0; i < 8; ++i) { const float d = xv[i] - mean; vv = fmaf(d, d, vv); }
      const float var = wave_sum(vv) * (1.f / 512.f);
      const float mb = rbf(mean), den = rbf(__builtin_sqrtf(rbf(rbf(var) + 1e-5f)));
      v4u xn;
#pragma unroll
      for (int k = 0; k < 4; ++k) xn[k] = cvtpk(rbf(xv[2 * k] - mb) / den, rbf(xv[2 * k + 1] - mb) / den);
      *(LAS v4u*)(L8 + L_XN + lane * 16) = xn;
      LDSW();
      float acc[8] = {0.f, 0.f, 0.f, 0.f, 0.f, 0.f, 0.f, 0.f};
#pragma unroll
      for (int i = 0; i < 4; ++i) fma8(acc, bf2f(((const LAS u16*)(L8 + L_XN))[r0 + 8 * i]), wv[i]);
#pragma unroll
      for (int k = 0; k < 8; ++k) { float t = acc[k]; t += dpp<ROR8>(t); acc[k] = xrow_sum(t); }
      if (lane < 8) {
        LAS f32x4* qp = (LAS f32x4*)(L8 + L_QP + c * 256 + lane * 32);
        qp[0] = f32x4{acc[0], acc[1], acc[2], acc[3]}; qp[1] = f32x4{acc[4], acc[5], acc[6], acc[7]};
      }
      LDSW();
      unsigned old = 0;
      if (lane == 0) old = __hip_atomic_fetch_add((LAS unsigned*)(L8 + L_MISC + 4 * M_QCNT), 1u, __ATOMIC_RELAXED, __HIP_MEMORY_SCOPE_WORKGROUP);
      old = __builtin_amdgcn_readfirstlane(old);
      CBAR();
      if (old == 3u) {
        if (lane < 16) {
          const LAS f32x4* qp = (const LAS f32x4*)(L8 + L_QP + lane * 16);
          const f32x4 qv = (qp[0] + qp[16]) + (qp[32] + qp[48]);
          ST16(__builtin_bit_cast(v4u, qv), W_QPG + ((size_t)(g * 4 + (s >> 3)) * 512 + 64 * (s & 7) + 4 * lane) * 4);
        }
        VMW0();
        while (MISC[M_CLR] == 0u) __builtin_amdgcn_s_sleep(1);
        CBAR();
        const u64 tag = ((u64)MISC[M_TAGHI] << 32) | (u64)MISC[M_TAGLO];
        if (lane == 0) __hip_atomic_store(QF + b, tag, RLX_AGENT);
      }
    }
    const int n = lane & 15, gq = lane >> 4;
    const int kbase = n * BLK + c * 256 + gq * 16;
    const int vbase = (lane >> 2) * BLK + c * 256 + (lane & 3) * 8;
    volatile LAS unsigned* FREEC = (volatile LAS unsigned*)(L8 + L_FREE + c * 4);
    bf16x8 qf[4];
    f32x4 O[8];
#pragma unroll
    for (int d = 0; d < 8; ++d) O[d] = f32x4{0.f, 0.f, 0.f, 0.f};
    float m = -INFINITY, l = 0.f;
#define READ_K(kf, fK) do { const int sK_ = (fK) % NSLOT; \
      while (FULL[sK_] < (unsigned)((fK) + 1)) { } \
      CBAR(); \
      _Pragma("unroll") for (int ks = 0; ks < 4; ++ks) kf[ks] = *(const LAS bf16x8*)(L8 + sK_ * SLOT + kbase + ks * 64); \
      LDSW(); FREEC[sK_ * 4] = (unsigned)((fK) + 1); \
      asm volatile("" : "+v"(kf[0]), "+v"(kf[1]), "+v"(kf[2]), "+v"(kf[3]) :: "memory"); } while (0)
#define READ_V(vf, fV) do { const int sV_ = (fV) % NSLOT; \
      while (FULL[sV_] < (unsigned)((fV) + 1)) { } \
      CBAR(); \
      _Pragma("unroll") for (int d = 0; d < 8; ++d) vf[d] = __builtin_amdgcn_ds_read_tr16_b64_v4i16((LAS s16x4*)(L8 + sV_ * SLOT + vbase + d * 32)); \
      LDSW(); FREEC[sV_ * 4] = (unsigned)((fV) + 1); \
      asm volatile("" : "+v"(vf[0]), "+v"(vf[1]), "+v"(vf[2]), "+v"(vf[3]), "+v"(vf[4]), "+v"(vf[5]), "+v"(vf[6]), "+v"(vf[7]) :: "memory"); } while (0)
    auto qk_softmax = [&](const bf16x8 (&kf)[4]) __attribute__((always_inline)) -> s16x4 {
      f32x4 S0 = {0.f, 0.f, 0.f, 0.f}, S1 = {0.f, 0.f, 0.f, 0.f};
      S0 = __builtin_amdgcn_mfma_f32_16x16x32_bf16(kf[0], qf[0], S0, 0, 0, 0);
      S1 = __builtin_amdgcn_mfma_f32_16x16x32_bf16(kf[1], qf[1], S1, 0, 0, 0);
      S0 = __builtin_amdgcn_mfma_f32_16x16x32_bf16(kf[2], qf[2], S0, 0, 0, 0);
      S1 = __builtin_amdgcn_mfma_f32_16x16x32_bf16(kf[3], qf[3], S1, 0, 0, 0);
      float sc[4];
#pragma unroll
      for (int r = 0; r < 4; ++r) sc[r] = rbf(rbf(S0[r] + S1[r]) * SCALE);
      const float pm = xrow_max(fmaxf(fmaxf(sc[0], sc[1]), fmaxf(sc[2], sc[3])));
      if (__any(pm > m)) {
        const float mn = fmaxf(m, pm);
        const float alpha = __builtin_amdgcn_exp2f((m - mn) * L2E);
        m = mn; l *= alpha;
        float al[4];
#pragma unroll
        for (int r = 0; r < 4; ++r) al[r] = __builtin_bit_cast(float, __builtin_amdgcn_readlane(__builtin_bit_cast(int, alpha), r));
#pragma unroll
        for (int d = 0; d < 8; ++d)
#pragma unroll
          for (int r = 0; r < 4; ++r) O[d][r] *= al[r];
      }
      float p[4];
#pragma unroll
      for (int r = 0; r < 4; ++r) p[r] = __builtin_amdgcn_exp2f((sc[r] - m) * L2E);
      l += (p[0] + p[1]) + (p[2] + p[3]);
      return __builtin_bit_cast(s16x4, v2u{cvtpk(p[0], p[1]), cvtpk(p[2], p[3])});
    };
    auto pv = [&](const s16x4 pf, const s16x4 (&vf)[8]) __attribute__((always_inline)) {
#pragma unroll
      for (int d = 0; d < 8; ++d) O[d] = __builtin_amdgcn_mfma_f32_16x16x16bf16_1k(pf, vf[d], O[d], 0, 0, 0);
    };
    {
      bf16x8 k0[4], k1[4], k2[4], k3[4]; s16x4 v0[8], v1[8], v2[8], v3[8];
      READ_K(k0, 0); READ_V(v0, 1);
      READ_K(k1, 2); READ_V(v1, 3);
      READ_K(k2, 4); READ_V(v2, 5);
      READ_K(k3, 6); READ_V(v3, 7);
      while (MISC[M_QRDY] == 0u) __builtin_amdgcn_s_sleep(1);
      CBAR();
#pragma unroll
      for (int ks = 0; ks < 4; ++ks) qf[ks] = *(const LAS bf16x8*)(L8 + L_QL + (n & 3) * 256 + ks * 64 + gq * 16);
      pv(qk_softmax(k0), v0); pv(qk_softmax(k1), v1); pv(qk_softmax(k2), v2); pv(qk_softmax(k3), v3);
    }
#pragma unroll 1
    for (int t = 4; t < NTILE; ++t) {
      bf16x8 kf[4]; s16x4 vf[8];
      READ_K(kf, 2 * t);
      const s16x4 pf = qk_softmax(kf);
      READ_V(vf, 2 * t + 1);
      pv(pf, vf);
    }
    const float lt = xrow_sum(l);
    {
      LAS unsigned* mrg = (LAS unsigned*)(L8 + L_MRG + c * 2048 + gq * 512 + n * 4);
      const unsigned mk0 = 0u - (unsigned)(gq & 1), mk1 = 0u - (unsigned)(gq >> 1);
#pragma unroll
      for (int d = 0; d < 8; ++d) {
        const unsigned a0 = __float_as_uint(O[d][0]), a1 = __float_as_uint(O[d][1]), a2 = __float_as_uint(O[d][2]), a3 = __float_as_uint(O[d][3]);
        const unsigned lo = (a1 & mk0) | (a0 & ~mk0), hi = (a3 & mk0) | (a2 & ~mk0);
        mrg[d * 16] = (hi & mk1) | (lo & ~mk1);
      }
      if (lane < 4) *(LAS v2u*)(L8 + L_ML + lane * 32 + c * 8) = v2u{__float_as_uint(m), __float_as_uint(lt)};
    }
    CBAR();
    if (lane == 0) __hip_atomic_fetch_add((LAS unsigned*)(L8 + L_MISC + 4 * M_CCNT), 1u, __ATOMIC_RELAXED, __HIP_MEMORY_SCOPE_WORKGROUP);
    {
      f32x4 ml01, ml23, ov[4];
      unsigned cnt;
      do {
        CBAR();
        cnt = MISC[M_CCNT];
        CBAR();
        ml01 = *(const LAS f32x4*)(L8 + L_ML + c * 32); ml23 = *(const LAS f32x4*)(L8 + L_ML + c * 32 + 16);
#pragma unroll
        for (int w = 0; w < 4; ++w) ov[w] = *(const LAS f32x4*)(L8 + L_MRG + w * 2048 + c * 512 + (lane & 31) * 16);
        asm volatile("" : "+v"(ml01), "+v"(ml23), "+v"(ov[0]), "+v"(ov[1]), "+v"(ov[2]), "+v"(ov[3]), "+v"(cnt));
      } while (cnt < 4u);
      const float M = fmaxf(fmaxf(ml01[0], ml01[2]), fmaxf(ml23[0], ml23[2]));
      const float w0 = __builtin_amdgcn_exp2f((ml01[0] - M) * L2E), w1v = __builtin_amdgcn_exp2f((ml01[2] - M) * L2E);
      const float w2v = __builtin_amdgcn_exp2f((ml23[0] - M) * L2E), w3v = __builtin_amdgcn_exp2f((ml23[2] - M) * L2E);
      const f32x4 o = (w0 * ov[0] + w1v * ov[1]) + (w2v * ov[2] + w3v * ov[3]);
      const float L = (w0 * ml01[1] + w1v * ml01[3]) + (w2v * ml23[1] + w3v * ml23[3]);
      const size_t off = W_PART + ((size_t)(4 * g + c) * 32 + s) * PROW;
      const v4u val = lane < 32 ? sane4(o) : v4u{sane(M), sane(L), 0u, 0u};
      if (lane <= 32) ST16(val, off + lane * 16);
    }
  } else if (wave == 5) {
    const u64 E = __hip_atomic_load((gu64*)(ws + W_E), RLX_AGENT);
    const v4u sv = {SENT, SENT, SENT, SENT};
#pragma unroll
    for (int h = 0; h < 4; ++h)
      if (lane < 33) ST16(sv, W_PART + ((size_t)(4 * g + h) * 32 + s) * PROW + lane * 16);
    if (lane < 16) ST16(sv, W_AP + ((size_t)hh * 512 + cb * 64) * 4 + lane * 16);
    else if (lane < 32) ST16(sv, W_BP + ((size_t)hh * 512 + cb * 64) * 4 + (lane - 16) * 16);
    else if (lane < 48 && s < 28) ST16(sv, W_GP + ((size_t)g * FF + s * 64) * 4 + (lane - 32) * 16);
    LDSW(); __builtin_amdgcn_s_barrier();
    VMW0();
    const u64 tag = E + 1;
    if (lane == 0) { MISC[M_TAGLO] = (unsigned)tag; MISC[M_TAGHI] = (unsigned)(tag >> 32); }
    LDSW();
    if (lane == 0) MISC[M_CLR] = 1u;
    for (unsigned spins = 0; spins < SPIN_CAP; ++spins) {
      const u64 v = __hip_atomic_load(QF + 32 * g + (lane & 31), RLX_AGENT);
      if (__all(v == tag)) break;
      __builtin_amdgcn_s_sleep(2);
    }
    CBAR();
    f32x4 qa[4], qb[4];
#pragma unroll
    for (int r = 0; r < 4; ++r) {
      qa[r] = __builtin_bit_cast(f32x4, LD16(W_QPG + ((size_t)(g * 4 + r) * 512 + 8 * lane) * 4));
      qb[r] = __builtin_bit_cast(f32x4, LD16(W_QPG + ((size_t)(g * 4 + r) * 512 + 8 * lane + 4) * 4));
    }
    const f32x4 q0 = (qa[0] + qa[1]) + (qa[2] + qa[3]), q1 = (qb[0] + qb[1]) + (qb[2] + qb[3]);
    *(LAS v4u*)(L8 + L_QL + lane * 16) = v4u{cvtpk(q0[0], q0[1]), cvtpk(q0[2], q0[3]), cvtpk(q1[0], q1[1]), cvtpk(q1[2], q1[3])};
    LDSW();
    if (lane == 0) MISC[M_QRDY] = 1u;
  } else if (wave == 6) {
    LDSW(); __builtin_amdgcn_s_barrier();
    if (s < 28) {
      while (MISC[M_QRDY] == 0u) __builtin_amdgcn_s_sleep(4);
      CBAR();
      const u16* W = (g >= 4) ? w3 : w1;
      const int r0 = 128 * (g & 3) + (lane >> 3);
      v4u a[16];
#pragma unroll
      for (int i = 0; i < 16; ++i) a[i] = ldnt(W + (size_t)(r0 + 8 * i) * FF + 64 * s + 8 * (lane & 7));
      float acc[8] = {0.f, 0.f, 0.f, 0.f, 0.f, 0.f, 0.f, 0.f};
#pragma unroll
      for (int i = 0; i < 16; ++i) fma8(acc, bf2f(((const LAS u16*)(L8 + L_XN))[r0 + 8 * i]), a[i]);
#pragma unroll
      for (int k = 0; k < 8; ++k) { float t = acc[k]; t += dpp<ROR8>(t); acc[k] = xrow_sum(t); }
      if (lane < 8) {
        const size_t off = W_GP + ((size_t)g * FF + s * 64 + lane * 8) * 4;
        ST16(sane4(f32x4{acc[0], acc[1], acc[2], acc[3]}), off);
        ST16(sane4(f32x4{acc[4], acc[5], acc[6], acc[7]}), off + 16);
      }
    }
  } else {
    LDSW(); __builtin_amdgcn_s_barrier();
    while (MISC[M_QRDY] == 0u) __builtin_amdgcn_s_sleep(4);
    CBAR();
    const u64 tag = ((u64)MISC[M_TAGHI] << 32) | (u64)MISC[M_TAGLO];
    v4u wv[7];
#pragma unroll
    for (int i = 0; i < 7; ++i) wv[i] = ldnt(w2 + (size_t)(56 * hh + 8 * i + (lane >> 3)) * 512 + 64 * cb + 8 * (lane & 7));
    for (unsigned spins = 0; spins < SPIN_CAP; ++spins) {
      bool ok = true;
#pragma unroll
      for (int i = 0; i < 4; ++i) ok &= __hip_atomic_load(QF + 64 * i + lane, RLX_AGENT) == tag;
      if (__all(ok)) break;
      __builtin_amdgcn_s_sleep(16);
    }
    CBAR();
    if (lane == 0) MISC[M_CHK] = 1u;
    unsigned gp[8];
    const int col = 56 * hh + (lane < 56 ? lane : 0);
    for (unsigned spins = 0; spins < SPIN_CAP; ++spins) {
      CBAR();
      bool ok = true;
#pragma unroll
      for (int r = 0; r < 8; ++r) { gp[r] = LD4(W_GP + ((size_t)r * FF + col) * 4); ok &= gp[r] != SENT; }
      if (__all(ok)) break;
      __builtin_amdgcn_s_sleep(16);
    }
    const float gb = rbf((__uint_as_float(gp[0]) + __uint_as_float(gp[1])) + (__uint_as_float(gp[2]) + __uint_as_float(gp[3])));
    const float ub = rbf((__uint_as_float(gp[4]) + __uint_as_float(gp[5])) + (__uint_as_float(gp[6]) + __uint_as_float(gp[7])));
    const float sg = rbf(gb / (1.f + __expf(-gb)));
    const float hv = rbf(sg * ub);
    float acc[8] = {0.f, 0.f, 0.f, 0.f, 0.f, 0.f, 0.f, 0.f};
#pragma unroll
    for (int i = 0; i < 7; ++i) fma8(acc, __shfl(hv, 8 * i + (lane >> 3)), wv[i]);
#pragma unroll
    for (int k = 0; k < 8; ++k) { float t = acc[k]; t += dpp<ROR8>(t); acc[k] = xrow_sum(t); }
    if (lane < 8) {
      const size_t off = W_BP + ((size_t)hh * 512 + cb * 64 + lane * 8) * 4;
      ST16(sane4(f32x4{acc[0], acc[1], acc[2], acc[3]}), off);
      ST16(sane4(f32x4{acc[4], acc[5], acc[6], acc[7]}), off + 16);
    }
  }

  while (MISC[M_CCNT] < 4u || MISC[M_CHK] == 0u) __builtin_amdgcn_s_sleep(2);
  CBAR();
  {
    const int c4 = tid & 31, sg = tid >> 5;
    const size_t pbase = W_PART + (size_t)hh * 32 * PROW;
    v4u o0 = {SENT, SENT, SENT, SENT}, o1 = {SENT, SENT, SENT, SENT}; v2u ml0 = {SENT, SENT}, ml1 = {SENT, SENT};
    for (unsigned spins = 0; spins < SPIN_CAP; ++spins) {
      CBAR();
      if (!valid4(o0) || !valid2(ml0)) { o0 = LD16(pbase + sg * PROW + c4 * 16); ml0 = LD8(pbase + sg * PROW + 512); }
      if (!valid4(o1) || !valid2(ml1)) { o1 = LD16(pbase + (sg + 16) * PROW + c4 * 16); ml1 = LD8(pbase + (sg + 16) * PROW + 512); }
      if (__all(valid4(o0) && valid4(o1) && valid2(ml0) && valid2(ml1))) break;
      __builtin_amdgcn_s_sleep(1);
    }
    const float m0 = __uint_as_float(ml0[0]), m1 = __uint_as_float(ml1[0]);
    const float Mw = x32_max(fmaxf(m0, m1));
    const float e0 = __builtin_amdgcn_exp2f((m0 - Mw) * L2E), e1 = __builtin_amdgcn_exp2f((m1 - Mw) * L2E);
    const float Lw = x32_sum(fmaf(e0, __uint_as_float(ml0[1]), e1 * __uint_as_float(ml1[1])));
    f32x4 o;
#pragma unroll
    for (int k = 0; k < 4; ++k) o[k] = x32_sum(fmaf(e0, __uint_as_float(o0[k]), e1 * __uint_as_float(o1[k])));
    if (lane < 32) *(LAS f32x4*)(L8 + L_RED + wave * 512 + c4 * 16) = o;
    if (lane == 0) *(LAS v2u*)(L8 + L_RML + wave * 8) = v2u{__float_as_uint(Mw), __float_as_uint(Lw)};
    VMW0();
    __syncthreads();
  }
  {
    const int j = lane >> 4, p = lane & 15, c2 = lane & 31, hf = lane >> 5;
    f32x4 mlj = *(const LAS f32x4*)(L8 + L_RML + j * 16);
    float r0 = *(const LAS float*)(L8 + L_RED + (2 * j) * 512 + (16 * wave + p) * 4);
    float r1 = *(const LAS float*)(L8 + L_RED + (2 * j + 1) * 512 + (16 * wave + p) * 4);
    unsigned ww[8];
#pragma unroll
    for (int i = 0; i < 8; ++i) ww[i] = *(const LAS unsigned*)(L8 + L_WO + (2 * wave + hf) * BLK + i * 128 + c2 * 4);
    asm volatile("" : "+v"(mlj), "+v"(r0), "+v"(r1), "+v"(ww[0]), "+v"(ww[1]), "+v"(ww[2]), "+v"(ww[3]), "+v"(ww[4]), "+v"(ww[5]), "+v"(ww[6]), "+v"(ww[7]));
    const float M = xrow_max(fmaxf(mlj[0], mlj[2]));
    const float f0 = __builtin_amdgcn_exp2f((mlj[0] - M) * L2E), f1 = __builtin_amdgcn_exp2f((mlj[2] - M) * L2E);
    const float L = xrow_sum(fmaf(f0, mlj[1], f1 * mlj[3]));
    const float ov = rbf(xrow_sum(fmaf(f0, r0, f1 * r1)) / L);
    const float ovr = dpp<ROR8>(ov);
    const float o2 = hf ? ovr : ov;
    float a0 = 0.f, a1 = 0.f;
#define WOSTEP(i) { const float oi = dpp<NEWBCAST + (i)>(o2); a0 = fmaf(oi, bf2f(ww[i] & 0xffffu), a0); a1 = fmaf(oi, bfhi(ww[i]), a1); }
    WOSTEP(0) WOSTEP(1) WOSTEP(2) WOSTEP(3) WOSTEP(4) WOSTEP(5) WOSTEP(6) WOSTEP(7)
#undef WOSTEP
    a0 = x32_sum(a0); a1 = x32_sum(a1);
    if (lane < 32) *(LAS v2u*)(L8 + L_RED2 + wave * 256 + c2 * 8) = v2u{__float_as_uint(a0), __float_as_uint(a1)};
    __syncthreads();
  }
  if (wave == 0) {
    {
      f32x4 a[8];
#pragma unroll
      for (int w = 0; w < 8; ++w) a[w] = *(const LAS f32x4*)(L8 + L_RED2 + w * 256 + (lane & 15) * 16);
      const f32x4 t = ((a[0] + a[1]) + (a[2] + a[3])) + ((a[4] + a[5]) + (a[6] + a[7]));
      if (lane < 16) ST16(sane4(t), W_AP + ((size_t)hh * 512 + cb * 64) * 4 + lane * 16);
    }
    const int col = 2 * b;
    const unsigned xw = *(const GAS unsigned*)(x + col);
    const size_t off = (lane < 32 ? W_AP : W_BP) + ((size_t)(lane & 31) * 512 + col) * 4;
    v2u v = {SENT, SENT};
    for (unsigned spins = 0; spins < SPIN_CAP; ++spins) {
      CBAR();
      if (!valid2(v)) v = LD8(off);
      if (__all(valid2(v))) break;
      __builtin_amdgcn_s_sleep(1);
    }
    float r0 = __uint_as_float(v[0]), r1 = __uint_as_float(v[1]);
    r0 += dpp<ROR1>(r0); r0 += dpp<ROR2>(r0); r0 += dpp<ROR4>(r0); r0 += dpp<ROR8>(r0); r0 = x16_sum(r0);
    r1 += dpp<ROR1>(r1); r1 += dpp<ROR2>(r1); r1 += dpp<ROR4>(r1); r1 += dpp<ROR8>(r1); r1 = x16_sum(r1);
    const float B0 = __shfl(r0, 32), B1 = __shfl(r1, 32);
    const float y0 = rbf(bf2f(xw & 0xffffu) + rbf(r0)) + rbf(B0);
    const float y1 = rbf(bfhi(xw) + rbf(r1)) + rbf(B1);
    if (lane == 0) {
      *(GAS unsigned*)(out + col) = cvtpk(y0, y1);
      if (b == 0) {
        const u64 tag = ((u64)MISC[M_TAGHI] << 32) | (u64)MISC[M_TAGLO];
        __hip_atomic_store((gu64*)(ws + W_E), tag, RLX_AGENT);
      }
    }
  }
}

extern "C" void kernel_launch(void* const* d_in, const int* in_sizes, int n_in,
                              void* d_out, int out_size, void* d_ws, size_t ws_size,
                              hipStream_t stream) {
  fused_kernel<<<256, 512, 0, stream>>>((const u16*)d_in[0], (const u16*)d_in[1], (const u16*)d_in[2], (const u16*)d_in[3], (const u16*)d_in[4],
                                        (const u16*)d_in[6], (const u16*)d_in[7], (const u16*)d_in[8], (u16*)d_out, (unsigned char*)d_ws);
}
```
